```python
import torch

def _softmax(x, axis=-1):
    return torch.softmax(x, dim=axis)
def _gelu(x):
    return torch.nn.functional.gelu(x, approximate="tanh")
def _silu(x):
    return torch.nn.functional.silu(x)
def _sigmoid(x):
    return torch.sigmoid(x)
def _layernorm(x, axis=-1, eps=1e-5):
    m = x.mean(dim=axis, keepdim=True)
    v = x.var(dim=axis, keepdim=True, unbiased=False)
    return (x - m) / torch.sqrt(v + eps)
def _scatter_add(tgt, idx, src, axis=0):
    return tgt.clone().index_add_(axis, idx, src)
def _segment_sum(src, seg, n):
    out = torch.zeros((n,) + src.shape[1:], dtype=src.dtype)
    return out.index_add_(0, seg, src)
def _pad(x, pw):
    flat = tuple(v for pair in reversed(pw) for v in pair)
    return torch.nn.functional.pad(x, flat)

kernel_name = 'standalone__normalfloat_lut_nf4__prompt'

def setup_inputs():

    _w = (lambda a: (lambda t: t.to(torch.bfloat16) if t.is_floating_point() else t)(torch.as_tensor(a)))
    B, S, D, F = 1, 2048, 576, 4608
    G = -1 if -1 > 0 else D
    D = (D // G) * G
    ng = D // G
    qw = torch.randint(0, 16, (D, F)).to(torch.int64)
    return {
        "x": _w((lambda *s: torch.randn(*s, dtype=torch.bfloat16) * 0.05)(B, S, D)),
        "qw": _w(qw),
        "absmax": _w(torch.abs((lambda *s: torch.randn(*s, dtype=torch.bfloat16) * 0.05)(ng, F)).to(float) + 0.01),
    }

def reference(x, qw, absmax):
    NF4 = torch.as_tensor([
        -1.0, -0.6961928009986877, -0.5250730514526367, -0.39491748809814453,
        -0.28444138169288635, -0.18477343022823334, -0.09105003625154495, 0.0,
        0.07958029955625534, 0.16093020141124725, 0.24611230194568634,
        0.33791524171829224, 0.44070982933044434, 0.5626170039176941,
        0.7229568362236023, 1.0,
    ])
    D, F = qw.shape[0], qw.shape[1]
    ng = absmax.shape[0]
    G = D // ng
    w = (lambda x, dt: x.to(dt))(NF4[qw], x.dtype)
    s = (lambda x, n, axis: torch.repeat_interleave(x, n, dim=axis))(absmax, G, axis=0)
    w = w * s
    return torch.einsum("bsd,df->bsf", x, w)
```

```cpp
#include <hip/hip_runtime.h>
#include <hip/hip_bf16.h>
typedef unsigned short u16;
__constant__ float NF4T[16] = {-1.0f, -0.6961928009986877f, -0.5250730514526367f, -0.39491748809814453f,
        -0.28444138169288635f, -0.18477343022823334f, -0.09105003625154495f, 0.0f,
        0.07958029955625534f, 0.16093020141124725f, 0.24611230194568634f,
        0.33791524171829224f, 0.44070982933044434f, 0.5626170039176941f,
        0.7229568362236023f, 1.0f};
__global__ void k(const __hip_bfloat16* x, const int* qw, const __hip_bfloat16* am, __hip_bfloat16* out) {
    int n = blockIdx.x * 256 + threadIdx.x;
    int m0 = blockIdx.y * 8;
    float lut[1];
    float acc[8] = {0,0,0,0,0,0,0,0};
    for (int kk = 0; kk < 576; ++kk) {
        int q = qw[kk * 4608 + n];
        float w = __bfloat162float(__float2bfloat16(NF4T[q]));
        #pragma unroll
        for (int i = 0; i < 8; ++i) acc[i] += __bfloat162float(x[(m0 + i) * 576 + kk]) * w;
    }
    float s = __bfloat162float(am[n]);
    #pragma unroll
    for (int i = 0; i < 8; ++i) out[(m0 + i) * 4608 + n] = __float2bfloat16(acc[i] * s);
}
extern "C" void kernel_launch(void* const* d_in, const int* in_sizes, int n_in,
                              void* d_out, int out_size, void* d_ws, size_t ws_size,
                              hipStream_t stream) {
    k<<<dim3(18, 256), 256, 0, stream>>>((const __hip_bfloat16*)d_in[0], (const int*)d_in[1], (const __hip_bfloat16*)d_in[2], (__hip_bfloat16*)d_out);
}
```

```cpp
#include <hip/hip_runtime.h>
#include <hip/hip_bf16.h>
typedef unsigned int u32;
typedef unsigned int u32x4 __attribute__((ext_vector_type(4)));
typedef unsigned int u32x2 __attribute__((ext_vector_type(2)));
typedef short bf16x8 __attribute__((ext_vector_type(8)));
typedef float f32x4 __attribute__((ext_vector_type(4)));
#define STMODE 1
constexpr int M = 2048, N = 4608, K = 576;
__constant__ unsigned short NF4B[16] = {0xBF80, 0xBF32, 0xBF06, 0xBECA, 0xBE92, 0xBE3D, 0xBDBA, 0x0000, 0x3DA3, 0x3E25, 0x3E7C, 0x3EAD, 0x3EE2, 0x3F10, 0x3F39, 0x3F80};
constexpr int LUT_OFF = 0, X_OFF = 32768, W_OFF = X_OFF + 65536, LDS_B = W_OFF + 2 * 18432;
__global__ __launch_bounds__(512, 2) void nf4_v1(const __hip_bfloat16* __restrict__ x, const int* __restrict__ qw, const __hip_bfloat16* __restrict__ am, __hip_bfloat16* __restrict__ out) {
    __shared__ __attribute__((aligned(16))) char smem[LDS_B];
    const int tid = threadIdx.x, l = tid & 63;
    const int w = __builtin_amdgcn_readfirstlane(tid >> 6);
    const int b = blockIdx.x, xcd = b & 7, j = b >> 3;
    const int nt = xcd * 4 + (j & 3), mt = j >> 2;
    const int kg = tid / 36, c = tid % 36;
    const bool wl = tid < 288;
    const u32x4* wp = (const u32x4*)(qw + (size_t)(kg * 8) * N + nt * 144 + c * 4);
    u32x4 st[8];
    auto loadW = [&](int ks) { if (wl) { for (int r = 0; r < 8; ++r) st[r] = wp[(size_t)(ks * 64 + r) * (N / 4)]; } };
    const char* xp = (const char*)x + (size_t)(mt * 256 + w * 32 + (l >> 3)) * (K * 2) + (((l & 7) ^ (l >> 3)) * 16);
    auto loadX = [&](int ks, int buf) {
        for (int i = 0; i < 4; ++i)
            __builtin_amdgcn_global_load_lds((const u32*)(xp + (size_t)i * 8 * K * 2 + ks * 128), (u32*)(smem + X_OFF + buf * 32768 + w * 4096 + i * 1024), 16, 0, 0);
    };
    loadW(0); loadX(0, 0);
    for (int i = 0; i < 4; ++i) {
        int e = w * 32 + i * 8 + (l >> 3);
        u32 v = (u32)NF4B[e & 15] | ((u32)NF4B[e >> 4] << 16);
        *(u32x4*)(smem + LUT_OFF + e * 128 + (l & 7) * 16) = u32x4{v, v, v, v};
    }
    __syncthreads();
    const u32 lb = (l & 31) * 4;
    auto deq = [&](int buf) {
        if (wl) {
            char* wb = smem + W_OFF + buf * 18432 + (((kg >> 2) * 9 + (c >> 2)) * 4 + (kg & 3)) * 256 + (c & 3) * 64;
            for (int t = 0; t < 4; ++t) {
                u32x4 o;
                for (int p = 0; p < 4; ++p) o[p] = *(const u32*)(smem + LUT_OFF + ((st[2 * p + 1][t] << 11) + (st[2 * p][t] << 7) + lb));
                *(u32x4*)(wb + t * 16) = o;
            }
        }
    };
    f32x4 acc[2][9] = {};
    deq(0);
    asm volatile("s_waitcnt vmcnt(0)" ::: "memory");
    __syncthreads();
    for (int ks = 0; ks < 9; ++ks) {
        int buf = ks & 1;
        if (ks < 8) { loadW(ks + 1); loadX(ks + 1, buf ^ 1); }
        #pragma unroll
        for (int s = 0; s < 2; ++s) {
            bf16x8 xf[2];
            for (int m = 0; m < 2; ++m) {
                int row = m * 16 + (l & 15), ch = s * 4 + (l >> 4);
                xf[m] = *(const bf16x8*)(smem + X_OFF + buf * 32768 + w * 4096 + row * 128 + ((ch ^ (row & 7)) * 16));
            }
            #pragma unroll
            for (int nb = 0; nb < 9; ++nb) {
                bf16x8 wf = *(const bf16x8*)(smem + W_OFF + buf * 18432 + (s * 9 + nb) * 1024 + l * 16);
                for (int m = 0; m < 2; ++m) acc[m][nb] = __builtin_amdgcn_mfma_f32_16x16x32_bf16(wf, xf[m], acc[m][nb], 0, 0, 0);
            }
        }
        if (ks < 8) deq(buf ^ 1);
        asm volatile("s_waitcnt vmcnt(0)" ::: "memory");
        __syncthreads();
    }
    char* ob = smem + w * (32 * 304);
    for (int nb = 0; nb < 9; ++nb) {
        u32x2 sc = *(const u32x2*)(am + nt * 144 + nb * 16 + (l >> 4) * 4);
        float s0 = __uint_as_float(sc.x << 16), s1 = __uint_as_float(sc.x & 0xffff0000u), s2 = __uint_as_float(sc.y << 16), s3 = __uint_as_float(sc.y & 0xffff0000u);
        for (int m = 0; m < 2; ++m) {
            __hip_bfloat16 h0 = __float2bfloat16(acc[m][nb][0] * s0), h1 = __float2bfloat16(acc[m][nb][1] * s1), h2 = __float2bfloat16(acc[m][nb][2] * s2), h3 = __float2bfloat16(acc[m][nb][3] * s3);
            u32x2 o = {(u32)__builtin_bit_cast(unsigned short, h0) | ((u32)__builtin_bit_cast(unsigned short, h1) << 16), (u32)__builtin_bit_cast(unsigned short, h2) | ((u32)__builtin_bit_cast(unsigned short, h3) << 16)};
            *(u32x2*)(ob + (m * 16 + (l & 15)) * 304 + nb * 32 + (l >> 4) * 8) = o;
        }
    }
    asm volatile("s_waitcnt lgkmcnt(0)" ::: "memory");
    for (int i = 0; i < 9; ++i) {
        int u = i * 64 + l, r = u / 18, cc = u % 18;
        u32x4 v = *(const u32x4*)(ob + r * 304 + cc * 16);
        char* gp = (char*)out + (size_t)(mt * 256 + w * 32 + r) * (N * 2) + nt * 288 + cc * 16;
        asm volatile("global_store_dwordx4 %0, %1, off sc1\n\ts_nop 1" :: "v"(gp), "v"(v) : "memory");
    }
}
extern "C" void kernel_launch(void* const* d_in, const int* in_sizes, int n_in, void* d_out, int out_size, void* d_ws, size_t ws_size, hipStream_t stream) {
    nf4_v1<<<256, 512, 0, stream>>>((const __hip_bfloat16*)d_in[0], (const int*)d_in[1], (const __hip_bfloat16*)d_in[2], (__hip_bfloat16*)d_out);
}
```

```cpp
#include <hip/hip_runtime.h>
#include <hip/hip_bf16.h>

__global__ __launch_bounds__(256, 1) __attribute__((aligned(4096)))
void nf4_gemm_kernel(const void* x, const void* qw, const void* absmax, void* out) {
    __shared__ __attribute__((aligned(16))) char smem[151552];
    asm volatile(
        "s_load_dwordx2 s[8:9], s[0:1], 0x8\n\t"
        "s_load_dwordx2 s[4:5], s[0:1], 0x0\n\t"
        "s_load_dwordx2 s[16:17], s[0:1], 0x10\n\t"
        "s_load_dwordx2 s[12:13], s[0:1], 0x18\n\t"
        "v_lshrrev_b32 v238, 6, v0\n\t"
        "v_and_b32 v12, 63, v0\n\t"
        "v_readfirstlane_b32 s20, v238\n\t"
        "s_lshr_b32 s25, s2, 3\n\t"
        "s_getpc_b64 s[96:97]\n\t"
        ".Lpc%=:\n\t"
        "s_mov_b32 s98, .Lend%=-.Lpc%=\n\t"
        "s_mov_b32 s99, 0x20000\n\t"
        "s_lshl2_add_u32 s26, s25, s20\n\t"
        "s_lshl_b32 s26, s26, 7\n\t"
        "v_lshl_add_u32 v246, v12, 14, s26\n\t"
        "s_and_b32 s21, s25, 1\n\t"
        "s_lshr_b32 s23, s25, 2\n\t"
        "s_and_b32 s26, s2, 7\n\t"
        "buffer_load_dword v250, v246, s[96:99], 0 offen\n\t"
        "s_mul_i32 s22, s26, 2304\n\t"
        "s_and_b32 s27, s25, 3\n\t"
        "s_lshl2_add_u32 s50, s27, s23\n\t"
        "s_add_u32 s50, s50, s26\n\t"
        "s_and_b32 s50, s50, 7\n\t"
        "s_add_u32 s50, s50, 1\n\t"
        "s_cmp_le_u32 s50, 2\n\t"
        "s_cselect_b32 s24, s50, 0\n\t"
        "v_and_b32 v13, 7, v0\n\t"
        "v_lshrrev_b32 v14, 3, v12\n\t"
        "s_mov_b32 s6, 2359296\n\t"
        "s_mov_b32 s7, 0x20000\n\t"
        "s_mov_b32 s10, 10616832\n\t"
        "s_mov_b32 s11, 0x74fac\n\t"
        "s_lshl_b32 s29, s27, 9\n\t"
        "s_add_u32 s29, s29, s22\n\t"
        "s_lshl_b32 s26, s20, 7\n\t"
        "s_add_u32 s26, s26, s29\n\t"
        "v_lshl_add_u32 v238, v13, 4, s26\n\t"
        "v_mul_u32_u24 v15, 36864, v14\n\t"
        "s_mul_i32 s25, s24, 1179648\n\t"
        "v_lshl_add_u32 v9, v15, 2, v238\n\t"
        "v_add_u32 v10, s25, v9\n\t"
        "s_mov_b32 s33, 18432\n\t"
        "s_mov_b32 s34, 36864\n\t"
        "s_mov_b32 s35, 55296\n\t"
        "s_mov_b32 s36, 73728\n\t"
        "s_mov_b32 s37, 92160\n\t"
        "s_mov_b32 s38, 110592\n\t"
        "s_mov_b32 s39, 129024\n\t"
        "v_and_b32 v240, 6, v14\n\t"
        "v_xor_b32 v240, v240, v13\n\t"
        "v_lshlrev_b32 v240, 4, v240\n\t"
        "v_mul_u32_u24 v241, 1152, v14\n\t"
        "s_lshl3_add_u32 s28, s23, s20\n\t"
        "s_mul_i32 s26, s28, 36864\n\t"
        "v_add3_u32 v248, v240, v241, s26\n\t"
        "v_lshl_add_u32 v247, s24, 7, v248\n\t"
        "s_mul_i32 s40, s21, 147456\n\t"
        "s_sub_u32 s44, 147456, s40\n\t"
        "s_add_u32 s41, s40, 8192\n\t"
        "s_add_u32 s42, s40, 16384\n\t"
        "s_add_u32 s43, s40, 24576\n\t"
        "s_add_u32 s45, s44, 8192\n\t"
        "s_add_u32 s46, s44, 16384\n\t"
        "s_add_u32 s47, s44, 24576\n\t"
        "s_mul_i32 s53, s20, 16384\n\t"
        "s_add_u32 s53, s53, 32768\n\t"
        "s_waitcnt lgkmcnt(0)\n\t"
        "buffer_load_format_d16_xyzw v[64:65], v10, s[8:11], 0 offen\n\t"
        "buffer_load_format_d16_xyzw v[66:67], v10, s[8:11], s33 offen\n\t"
        "buffer_load_format_d16_xyzw v[68:69], v10, s[8:11], s34 offen\n\t"
        "buffer_load_format_d16_xyzw v[70:71], v10, s[8:11], s35 offen\n\t"
        "v_and_b32 v243, 31, v0\n\t"
        "v_lshlrev_b32 v1, 2, v243\n\t"
        "s_movk_i32 s48, 0x80\n\t"
        "s_sub_u32 s52, s50, 2\n\t"
        "s_max_i32 s52, s52, 0\n\t"
        "s_movk_i32 s74, 0x10e\n\t"
        "s_cmp_eq_u32 s50, 1\n\t"
        "s_cselect_b32 s74, 15, s74\n\t"
        "s_mov_b32 s75, 0\n\t"
        "s_lshl_b32 s26, s52, 2\n\t"
        "s_lshl_b64 s[74:75], s[74:75], s26\n\t"
        "s_sub_u32 s72, 0x76543210, s74\n\t"
        "s_subb_u32 s73, 8, s75\n\t"
        "v_and_b32 v239, 15, v12\n\t"
        "v_lshrrev_b32 v240, 4, v12\n\t"
        "v_and_b32 v242, 6, v12\n\t"
        "v_xor_b32 v242, v242, v240\n\t"
        "v_lshlrev_b32 v244, 4, v242\n\t"
        "v_lshl_add_u32 v244, v239, 7, v244\n\t"
        "v_add_u32 v7, s53, v244\n\t"
        "buffer_load_format_d16_xyzw v[72:73], v10, s[8:11], s36 offen\n\t"
        "v_xor_b32 v8, 64, v7\n\t"
        "s_mov_b32 s80, 0xbf32bf80\n\t"
        "s_mov_b32 s81, 0xbecabf06\n\t"
        "s_mov_b32 s82, 0xbe3dbe92\n\t"
        "s_mov_b32 s83, 0xbdba\n\t"
        "s_mov_b32 s84, 0x3e253da3\n\t"
        "s_mov_b32 s85, 0x3ead3e7c\n\t"
        "s_mov_b32 s86, 0x3f103ee2\n\t"
        "s_mov_b32 s87, 0x3f803f39\n\t"
        "v_and_b32 v241, 3, v14\n\t"
        "v_lshlrev_b32 v239, 4, v241\n\t"
        "v_cmp_gt_u32 vcc, 4, v14\n\t"
        "v_lshrrev_b64 v[48:49], v239, s[80:81]\n\t"
        "v_lshrrev_b64 v[50:51], v239, s[82:83]\n\t"
        "v_lshrrev_b64 v[52:53], v239, s[84:85]\n\t"
        "v_lshrrev_b64 v[54:55], v239, s[86:87]\n\t"
        "v_cndmask_b32 v242, v50, v48, vcc\n\t"
        "v_cndmask_b32 v244, v54, v52, vcc\n\t"
        "v_and_b32 v242, 0xffff, v242\n\t"
        "v_and_b32 v244, 0xffff, v244\n\t"
        "s_bitcmp0_b32 s20, 1\n\t"
        "s_cselect_b64 s[80:81], s[80:81], s[84:85]\n\t"
        "s_cselect_b64 s[82:83], s[82:83], s[86:87]\n\t"
        "s_bitcmp0_b32 s20, 0\n\t"
        "s_cselect_b64 s[80:81], s[80:81], s[82:83]\n\t"
        "buffer_load_format_d16_xyzw v[74:75], v10, s[8:11], s37 offen\n\t"
        "s_lshl_b32 s88, s80, 16\n\t"
        "s_and_b32 s89, s80, 0xffff0000\n\t"
        "s_lshl_b32 s90, s81, 16\n\t"
        "s_and_b32 s91, s81, 0xffff0000\n\t"
        "v_lshlrev_b32 v238, 4, v12\n\t"
        "s_lshl_b32 s25, s20, 13\n\t"
        "v_add_u32 v239, s25, v238\n\t"
        "v_or_b32 v48, s88, v242\n\t"
        "v_or_b32 v49, s88, v242\n\t"
        "v_or_b32 v50, s88, v242\n\t"
        "v_or_b32 v51, s88, v242\n\t"
        "ds_write_b128 v239, v[48:51]\n\t"
        "buffer_load_format_d16_xyzw v[76:77], v10, s[8:11], s38 offen\n\t"
        "v_or_b32 v52, s88, v244\n\t"
        "v_or_b32 v53, s88, v244\n\t"
        "v_or_b32 v54, s88, v244\n\t"
        "v_or_b32 v55, s88, v244\n\t"
        "v_add_u32 v4, 98304, v238\n\t"
        "v_lshrrev_b32 v243, 2, v14\n\t"
        "ds_write_b128 v239, v[52:55] offset:1024\n\t"
        "v_or_b32 v56, s89, v242\n\t"
        "v_or_b32 v57, s89, v242\n\t"
        "v_or_b32 v58, s89, v242\n\t"
        "v_or_b32 v59, s89, v242\n\t"
        "s_lshr_b32 s25, s20, 1\n\t"
        "s_lshl_b32 s25, s25, 6\n\t"
        "buffer_load_format_d16_xyzw v[78:79], v10, s[8:11], s39 offen\n\t"
        "ds_write_b128 v239, v[56:59] offset:2048\n\t"
        "s_add_u32 m0, s53, 0\n\t"
        "v_or_b32 v60, s89, v244\n\t"
        "v_or_b32 v61, s89, v244\n\t"
        "v_or_b32 v62, s89, v244\n\t"
        "v_or_b32 v63, s89, v244\n\t"
        "s_and_b32 s26, s20, 1\n\t"
        "s_add_u32 s25, s25, s26\n\t"
        "ds_write_b128 v239, v[60:63] offset:3072\n\t"
        "buffer_load_dwordx4 v247, s[4:7], s40 offen lds\n\t"
        "v_or_b32 v48, s90, v242\n\t"
        "v_or_b32 v49, s90, v242\n\t"
        "v_or_b32 v50, s90, v242\n\t"
        "v_or_b32 v51, s90, v242\n\t"
        "s_lshl_b32 s25, s25, 7\n\t"
        "s_add_u32 s25, s25, 102400\n\t"
        "ds_write_b128 v239, v[48:51] offset:4096\n\t"
        "v_or_b32 v52, s90, v244\n\t"
        "v_or_b32 v53, s90, v244\n\t"
        "v_or_b32 v54, s90, v244\n\t"
        "v_or_b32 v55, s90, v244\n\t"
        "v_lshl_add_u32 v2, v13, 4, s25\n\t"
        "v_lshl_add_u32 v2, v241, 8, v2\n\t"
        "buffer_load_dwordx4 v247, s[4:7], s41 offen offset:1024 lds\n\t"
        "ds_write_b128 v239, v[52:55] offset:5120\n\t"
        "v_or_b32 v56, s91, v242\n\t"
        "v_or_b32 v57, s91, v242\n\t"
        "v_or_b32 v58, s91, v242\n\t"
        "v_or_b32 v59, s91, v242\n\t"
        "v_lshl_add_u32 v2, v243, 12, v2\n\t"
        "ds_write_b128 v239, v[56:59] offset:6144\n\t"
        "buffer_load_dwordx4 v247, s[4:7], s42 offen offset:2048 lds\n\t"
        "v_or_b32 v60, s91, v244\n\t"
        "v_or_b32 v61, s91, v244\n\t"
        "v_or_b32 v62, s91, v244\n\t"
        "v_or_b32 v63, s91, v244\n\t"
        "ds_write_b128 v239, v[60:63] offset:7168\n\t"
        "s_waitcnt vmcnt(11) lgkmcnt(0)\n\t"
        "s_barrier\n\t"
        "s_lshr_b32 s27, s27, 1\n\t"
        "s_lshl_b32 s27, s27, 7\n\t"
        "s_add_u32 s27, s27, s22\n\t"
        "s_add_u32 s27, s27, 2048\n\t"
        "s_mov_b64 s[76:77], s[8:9]\n\t"
        "s_mov_b32 s79, 0x20000\n\t"
        "s_lshl2_add_u32 s25, s50, s20\n\t"
        "s_lshl1_add_u32 s25, s25, s21\n\t"
        "s_mul_i32 s25, s25, 147456\n\t"
        "s_add_u32 s25, s25, s27\n\t"
        "v_mul_u32_u24 v239, 18432, v13\n\t"
        "s_lshl1_add_u32 s26, s50, s21\n\t"
        "s_lshl2_add_u32 s26, s26, s20\n\t"
        "s_mul_i32 s26, s26, 73728\n\t"
        "s_add_u32 s26, s26, s27\n\t"
        "s_sub_u32 s26, s26, 589824\n\t"
        "s_cmp_le_u32 s50, 2\n\t"
        "s_cselect_b32 s26, s26, 0x8000000\n\t"
        "s_cselect_b32 s52, 2, s52\n\t"
        "buffer_load_dwordx4 v247, s[4:7], s43 offen offset:3072 lds\n\t"
        "s_add_u32 m0, s53, 4096\n\t"
        "v_add_u32 v240, 4, v12\n\t"
        "v_lshrrev_b32 v240, 4, v240\n\t"
        "v_lshl_add_u32 v239, v240, 27, v239\n\t"
        "v_cmp_gt_u32 vcc, 8, v12\n\t"
        "v_add_u32 v240, s26, v239\n\t"
        "v_add_u32 v239, s25, v239\n\t"
        "v_cndmask_b32 v245, v240, v239, vcc\n\t"
        "s_lshl_b32 s49, s20, 3\n\t"
        "s_or_b32 s49, s49, 0x80000\n\t"
        "s_bfe_u32 s74, ((((.Lp4_%=-.Lpc%=)&1023)>>2)<<0)+((((.Lp8_%=-.Lpc%=)&1023)>>2)<<8)+((((.Lp12_%=-.Lpc%=)&1023)>>2)<<16)+((((.Lp16_%=-.Lpc%=)&1023)>>2)<<24), s49\n\t"
        "s_bfe_u32 s75, (((.Lp4_%=-.Lpc%=)>>10)<<0)+(((.Lp8_%=-.Lpc%=)>>10)<<8)+(((.Lp12_%=-.Lpc%=)>>10)<<16)+(((.Lp16_%=-.Lpc%=)>>10)<<24), s49\n\t"
        "s_lshl_b32 s75, s75, 10\n\t"
        "s_lshl2_add_u32 s74, s74, s75\n\t"
        "s_add_u32 s74, s74, s96\n\t"
        "s_addc_u32 s75, s97, 0\n\t"
        "s_mov_b64 exec, 0\n\t"
        "s_setpc_b64 s[74:75]\n\t"
        ".Lret0_%=:\n\t"
        "s_mov_b64 exec, -1\n\t"
        "buffer_load_dwordx4 v247, s[4:7], s44 offen lds\n\t"
        "s_lshl2_add_u32 s25, s24, s20\n\t"
        "s_waitcnt vmcnt(9)\n\t"
        "v_lshl_or_b32 v64, v66, 4, v64\n\t"
        "v_mad_u32_u16 v48, v64, s48, v1\n\t"
        "v_mad_u32_u16 v52, v64, s48, v1 op_sel:[1,0,0,0]\n\t"
        "v_lshl_or_b32 v65, v67, 4, v65\n\t"
        "v_mad_u32_u16 v56, v65, s48, v1\n\t"
        "v_mad_u32_u16 v60, v65, s48, v1 op_sel:[1,0,0,0]\n\t"
        "v_lshl_or_b32 v68, v70, 4, v68\n\t"
        "v_mad_u32_u16 v49, v68, s48, v1\n\t"
        "v_mad_u32_u16 v53, v68, s48, v1 op_sel:[1,0,0,0]\n\t"
        "v_lshl_or_b32 v69, v71, 4, v69\n\t"
        "v_mad_u32_u16 v57, v69, s48, v1\n\t"
        "v_mad_u32_u16 v61, v69, s48, v1 op_sel:[1,0,0,0]\n\t"
        "s_mul_i32 s25, s25, 294912\n\t"
        "s_add_u32 s25, s25, s27\n\t"
        "buffer_load_dwordx4 v247, s[4:7], s45 offen offset:1024 lds\n\t"
        "ds_read_b32 v48, v48\n\t"
        "v_lshl_add_u32 v242, v13, 4, s25\n\t"
        "v_add_u32 v15, v15, v242\n\t"
        "s_mov_b32 s18, 9216\n\t"
        "s_mov_b32 s19, 0x20000\n\t"
        "ds_read_b32 v52, v52\n\t"
        "v_and_b32 v239, 15, v12\n\t"
        "s_lshr_b32 s25, s29, 1\n\t"
        "s_lshr_b32 s26, s27, 1\n\t"
        "ds_read_b32 v56, v56\n\t"
        "v_lshl_add_u32 v17, v239, 3, s25\n\t"
        "v_lshl_add_u32 v18, v239, 2, s26\n\t"
        "v_lshrrev_b32 v240, 4, v12\n\t"
        "s_mul_i32 s25, s28, 294912\n\t"
        "v_mul_u32_u24 v240, 36864, v240\n\t"
        "v_add3_u32 v5, v17, v240, s25\n\t"
        "v_add3_u32 v6, v18, v240, s25\n\t"
        "s_lshl_b32 s25, s20, 12\n\t"
        "s_add_u32 s25, s25, 102400\n\t"
        "buffer_load_dwordx4 v247, s[4:7], s46 offen offset:2048 lds\n\t"
        "ds_read_b32 v60, v60\n\t"
        "v_lshl_add_u32 v3, v13, 5, s25\n\t"
        "v_lshl_add_u32 v3, v241, 8, v3\n\t"
        "v_lshl_add_u32 v3, v243, 11, v3\n\t"
        "s_lshr_b32 s25, s20, 1\n\t"
        "s_and_b32 s26, s20, 1\n\t"
        "ds_read_b32 v49, v49\n\t"
        "s_lshl2_add_u32 s25, s25, s26\n\t"
        "ds_read_b32 v53, v53\n\t"
        "s_lshl_b32 s25, s25, 9\n\t"
        "ds_read_b32 v57, v57\n\t"
        "s_add_u32 s25, s25, 98304\n\t"
        "ds_read_b32 v61, v61\n\t"
        "v_lshl_add_u32 v16, v13, 5, s25\n\t"
        "v_lshl_add_u32 v16, v241, 2, v16\n\t"
        "v_lshl_add_u32 v16, v243, 8, v16\n\t"
        "s_sub_u32 s51, s27, s29\n\t"
        "s_sub_u32 s51, s51, 512\n\t"
        "buffer_load_dwordx4 v247, s[4:7], s47 offen offset:3072 lds\n\t"
        "s_bfe_u32 s55, s72, 0x40004\n\t"
        "s_mul_i32 s56, s55, 1179648\n\t"
        "v_add_u32 v10, s56, v9\n\t"
        "s_bfe_u32 s63, 0x20201c1c, s49\n\t"
        "s_lshr_b64 s[74:75], s[72:73], s63\n\t"
        "s_bfe_i32 s63, 0xfefe0000, s49\n\t"
        "s_and_b32 s74, s74, 15\n\t"
        "s_lshl_b32 s63, s63, 7\n\t"
        "s_mul_i32 s74, s74, 1179648\n\t"
        "s_add_u32 s63, s63, s74\n\t"
        "s_bfe_u32 s64, 0x20201c1c, s49\n\t"
        "s_lshr_b64 s[74:75], s[72:73], s64\n\t"
        "s_bfe_i32 s64, 0x202, s49\n\t"
        "s_and_b32 s74, s74, 15\n\t"
        "buffer_load_format_d16_xyzw v[80:81], v10, s[8:11], 0 offen\n\t"
        "s_lshl_b32 s64, s64, 7\n\t"
        "s_mul_i32 s74, s74, 1179648\n\t"
        "s_add_u32 s64, s64, s74\n\t"
        "s_bfe_u32 s65, 0x100c0804, s49\n\t"
        "s_lshr_b64 s[74:75], s[72:73], s65\n\t"
        "s_bfe_i32 s65, 0x1020304, s49\n\t"
        "s_and_b32 s74, s74, 15\n\t"
        "s_lshl_b32 s65, s65, 7\n\t"
        "s_add_u32 s65, s65, s51\n\t"
        "s_mul_i32 s74, s74, 1179648\n\t"
        "s_add_u32 s65, s65, s74\n\t"
        "s_bfe_u32 s66, 0x201c1814, s49\n\t"
        "s_lshr_b64 s[74:75], s[72:73], s66\n\t"
        "s_bfe_i32 s66, 0x1020304, s49\n\t"
        "buffer_load_format_d16_xyzw v[82:83], v10, s[8:11], s33 offen\n\t"
        "s_and_b32 s74, s74, 15\n\t"
        "s_lshl_b32 s66, s66, 7\n\t"
        "s_add_u32 s66, s66, s51\n\t"
        "s_mul_i32 s74, s74, 1179648\n\t"
        "s_add_u32 s66, s66, s74\n\t"
        "s_mov_b32 s14, 18874368\n\t"
        "s_mov_b32 s15, 0x20000\n\t"
        "s_mul_i32 s57, s21, 1179648\n\t"
        "s_sub_u32 s70, 1179648, s57\n\t"
        "s_add_u32 s58, s57, 9216\n\t"
        "s_waitcnt vmcnt(10) lgkmcnt(0)\n\t"
        "v_lshl_or_b32 v72, v74, 4, v72\n\t"
        "v_mad_u32_u16 v50, v72, s48, v1\n\t"
        "v_mad_u32_u16 v54, v72, s48, v1 op_sel:[1,0,0,0]\n\t"
        "v_lshl_or_b32 v73, v75, 4, v73\n\t"
        "v_mad_u32_u16 v58, v73, s48, v1\n\t"
        "v_mad_u32_u16 v62, v73, s48, v1 op_sel:[1,0,0,0]\n\t"
        "v_lshl_or_b32 v76, v78, 4, v76\n\t"
        "v_mad_u32_u16 v51, v76, s48, v1\n\t"
        "v_mad_u32_u16 v55, v76, s48, v1 op_sel:[1,0,0,0]\n\t"
        "s_add_u32 s59, s57, 18432\n\t"
        "s_add_u32 s60, s57, 27648\n\t"
        "buffer_load_format_d16_xyzw v[84:85], v10, s[8:11], s34 offen\n\t"
        "ds_read_b32 v50, v50\n\t"
        "v_lshl_or_b32 v77, v79, 4, v77\n\t"
        "v_mad_u32_u16 v59, v77, s48, v1\n\t"
        "v_mad_u32_u16 v63, v77, s48, v1 op_sel:[1,0,0,0]\n\t"
        "s_add_u32 s61, s57, 147456\n\t"
        "s_add_u32 s62, s57, 156672\n\t"
        "ds_read_b32 v54, v54\n\t"
        "s_add_u32 s68, s57, 165888\n\t"
        "ds_read_b32 v58, v58\n\t"
        "s_add_u32 s69, s57, 175104\n\t"
        "ds_read_b32 v62, v62\n\t"
        "s_add_u32 s71, s70, 9216\n\t"
        "ds_read_b32 v51, v51\n\t"
        "s_add_u32 s88, s70, 18432\n\t"
        "buffer_load_format_d16_xyzw v[86:87], v10, s[8:11], s35 offen\n\t"
        "ds_read_b32 v55, v55\n\t"
        "s_add_u32 s89, s70, 27648\n\t"
        "ds_read_b32 v59, v59\n\t"
        "s_add_u32 s90, s70, 147456\n\t"
        "ds_read_b32 v63, v63\n\t"
        "s_add_u32 s91, s70, 156672\n\t"
        "s_add_u32 s54, s70, 165888\n\t"
        "s_add_u32 s3, s70, 175104\n\t"
        "buffer_load_format_d16_xyzw v[88:89], v10, s[8:11], s36 offen\n\t"
        "s_waitcnt vmcnt(11) lgkmcnt(3)\n\t"
        "ds_write_b128 v2, v[48:51]\n\t"
        "s_waitcnt lgkmcnt(1)\n\t"
        "ds_write_b128 v2, v[52:55] offset:1024\n\t"
        "buffer_load_format_d16_xyzw v[90:91], v10, s[8:11], s37 offen\n\t"
        "ds_write_b128 v2, v[56:59] offset:2048\n\t"
        "ds_write_b128 v2, v[60:63] offset:3072\n\t"
        "buffer_load_format_d16_xyzw v[92:93], v10, s[8:11], s38 offen\n\t"
        "s_waitcnt vmcnt(11) lgkmcnt(3)\n\t"
        "ds_read_b128 a[0:3], v7\n\t"
        "ds_read_b128 a[4:7], v8\n\t"
        "ds_read_b128 a[8:11], v7 offset:2048\n\t"
        "ds_read_b128 a[12:15], v8 offset:2048\n\t"
        "buffer_load_format_d16_xyzw v[94:95], v10, s[8:11], s39 offen\n\t"
        "v_lshl_add_u32 v247, s55, 7, v248\n\t"
        "s_add_u32 m0, s53, 8192\n\t"
        "s_waitcnt vmcnt(11) lgkmcnt(4)\n\t"
        "s_barrier\n\t"
        "ds_read_b128 v[20:23], v4 offset:4096\n\t"
        "ds_read_b128 v[24:27], v4 offset:8192\n\t"
        "ds_read_b128 v[28:31], v4 offset:5120\n\t"
        "ds_read_b128 v[32:35], v4 offset:9216\n\t"
        "buffer_load_dwordx4 v247, s[4:7], s40 offen lds\n\t"
        "ds_read_b128 v[36:39], v4 offset:6144\n\t"
        "ds_read_b128 v[40:43], v4 offset:10240\n\t"
        "ds_read_b128 v[44:47], v4 offset:7168\n\t"
        "s_waitcnt vmcnt(11) lgkmcnt(5)\n\t"
        "v_mfma_f32_16x16x32_bf16 v[96:99], a[0:3], v[20:23], 0\n\t"
        "buffer_load_dwordx4 v247, s[4:7], s41 offen offset:1024 lds\n\t"
        "v_mfma_f32_16x16x32_bf16 v[96:99], a[4:7], v[24:27], v[96:99]\n\t"
        "v_mfma_f32_16x16x32_bf16 v[104:107], a[12:15], v[24:27], 0\n\t"
        "v_mfma_f32_16x16x32_bf16 v[104:107], a[8:11], v[20:23], v[104:107]\n\t"
        "buffer_load_dwordx4 v247, s[4:7], s42 offen offset:2048 lds\n\t"
        "s_waitcnt vmcnt(11) lgkmcnt(0)\n\t"
        "ds_read_b128 a[16:19], v7 offset:4096\n\t"
        "ds_read_b128 a[20:23], v8 offset:4096\n\t"
        "ds_read_b128 a[24:27], v7 offset:6144\n\t"
        "ds_read_b128 a[28:31], v8 offset:6144\n\t"
        "buffer_load_dwordx4 v247, s[4:7], s43 offen offset:3072 lds\n\t"
        "s_cmp_eq_u32 s52, 1\n\t"
        "s_cselect_b32 s78, s10, 0\n\t"
        "s_waitcnt vmcnt(11) lgkmcnt(2)\n\t"
        "v_mfma_f32_16x16x32_bf16 v[112:115], a[16:19], v[20:23], 0\n\t"
        "v_mfma_f32_16x16x32_bf16 v[112:115], a[20:23], v[24:27], v[112:115]\n\t"
        "s_waitcnt lgkmcnt(0)\n\t"
        "v_mfma_f32_16x16x32_bf16 v[120:123], a[28:31], v[24:27], 0\n\t"
        "buffer_load_dword v250, v245, s[76:79], 0 offen\n\t"
        "ds_read_b128 v[24:27], v4 offset:11264\n\t"
        "s_add_u32 m0, s53, 12288\n\t"
        "v_mfma_f32_16x16x32_bf16 v[120:123], a[24:27], v[20:23], v[120:123]\n\t"
        "ds_read_b128 v[20:23], v4 offset:12288\n\t"
        "v_mfma_f32_16x16x32_bf16 v[124:127], a[24:27], v[28:31], 0\n\t"
        "v_mfma_f32_16x16x32_bf16 v[124:127], a[28:31], v[32:35], v[124:127]\n\t"
        "buffer_load_dwordx4 v247, s[4:7], s44 offen lds\n\t"
        "v_mfma_f32_16x16x32_bf16 v[116:119], a[20:23], v[32:35], 0\n\t"
        "v_mfma_f32_16x16x32_bf16 v[116:119], a[16:19], v[28:31], v[116:119]\n\t"
        "v_mfma_f32_16x16x32_bf16 v[108:111], a[8:11], v[28:31], 0\n\t"
        "v_mfma_f32_16x16x32_bf16 v[108:111], a[12:15], v[32:35], v[108:111]\n\t"
        "buffer_load_dwordx4 v247, s[4:7], s45 offen offset:1024 lds\n\t"
        "s_waitcnt vmcnt(11) lgkmcnt(0)\n\t"
        "v_lshl_or_b32 v80, v82, 4, v80\n\t"
        "v_mad_u32_u16 v48, v80, s48, v1\n\t"
        "v_mfma_f32_16x16x32_bf16 v[100:103], a[4:7], v[32:35], 0\n\t"
        "ds_read_b128 v[32:35], v4 offset:16384\n\t"
        "v_mad_u32_u16 v52, v80, s48, v1 op_sel:[1,0,0,0]\n\t"
        "v_lshl_or_b32 v81, v83, 4, v81\n\t"
        "v_mad_u32_u16 v56, v81, s48, v1\n\t"
        "v_mad_u32_u16 v60, v81, s48, v1 op_sel:[1,0,0,0]\n\t"
        "v_mfma_f32_16x16x32_bf16 v[100:103], a[0:3], v[28:31], v[100:103]\n\t"
        "ds_read_b128 v[28:31], v4 offset:13312\n\t"
        "v_lshl_or_b32 v84, v86, 4, v84\n\t"
        "v_mad_u32_u16 v49, v84, s48, v1\n\t"
        "v_mad_u32_u16 v53, v84, s48, v1 op_sel:[1,0,0,0]\n\t"
        "v_lshl_or_b32 v85, v87, 4, v85\n\t"
        "v_mfma_f32_16x16x32_bf16 v[128:131], a[0:3], v[36:39], 0\n\t"
        "ds_read_b32 v48, v48\n\t"
        "v_mad_u32_u16 v57, v85, s48, v1\n\t"
        "v_mad_u32_u16 v61, v85, s48, v1 op_sel:[1,0,0,0]\n\t"
        "v_mfma_f32_16x16x32_bf16 v[128:131], a[4:7], v[40:43], v[128:131]\n\t"
        "buffer_load_dwordx4 v247, s[4:7], s46 offen offset:2048 lds\n\t"
        "ds_read_b32 v52, v52\n\t"
        "ds_read_b32 v56, v56\n\t"
        "v_mfma_f32_16x16x32_bf16 v[136:139], a[12:15], v[40:43], 0\n\t"
        "ds_read_b32 v60, v60\n\t"
        "ds_read_b32 v49, v49\n\t"
        "ds_read_b32 v53, v53\n\t"
        "v_mfma_f32_16x16x32_bf16 v[136:139], a[8:11], v[36:39], v[136:139]\n\t"
        "ds_read_b32 v57, v57\n\t"
        "ds_read_b32 v61, v61\n\t"
        "v_mfma_f32_16x16x32_bf16 v[144:147], a[16:19], v[36:39], 0\n\t"
        "buffer_load_dwordx4 v247, s[4:7], s47 offen offset:3072 lds\n\t"
        "s_bfe_u32 s55, s72, 0x40008\n\t"
        "s_mul_i32 s56, s55, 1179648\n\t"
        "v_add_u32 v10, s56, v9\n\t"
        "v_mfma_f32_16x16x32_bf16 v[144:147], a[20:23], v[40:43], v[144:147]\n\t"
        "v_mfma_f32_16x16x32_bf16 v[152:155], a[28:31], v[40:43], 0\n\t"
        "ds_read_b128 v[40:43], v4 offset:17408\n\t"
        "v_mfma_f32_16x16x32_bf16 v[152:155], a[24:27], v[36:39], v[152:155]\n\t"
        "ds_read_b128 v[36:39], v4 offset:14336\n\t"
        "buffer_load_format_d16_xyzw v[64:65], v10, s[8:11], 0 offen\n\t"
        "v_mfma_f32_16x16x32_bf16 v[156:159], a[24:27], v[44:47], 0\n\t"
        "v_mfma_f32_16x16x32_bf16 v[156:159], a[28:31], v[24:27], v[156:159]\n\t"
        "v_mfma_f32_16x16x32_bf16 v[148:151], a[20:23], v[24:27], 0\n\t"
        "v_mfma_f32_16x16x32_bf16 v[148:151], a[16:19], v[44:47], v[148:151]\n\t"
        "buffer_load_format_d16_xyzw v[66:67], v10, s[8:11], s33 offen\n\t"
        "s_waitcnt vmcnt(11) lgkmcnt(0)\n\t"
        "v_lshl_or_b32 v88, v90, 4, v88\n\t"
        "v_mad_u32_u16 v50, v88, s48, v1\n\t"
        "v_mfma_f32_16x16x32_bf16 v[140:143], a[8:11], v[44:47], 0\n\t"
        "ds_read_b32 v50, v50\n\t"
        "v_mad_u32_u16 v54, v88, s48, v1 op_sel:[1,0,0,0]\n\t"
        "v_lshl_or_b32 v89, v91, 4, v89\n\t"
        "v_mad_u32_u16 v58, v89, s48, v1\n\t"
        "v_mad_u32_u16 v62, v89, s48, v1 op_sel:[1,0,0,0]\n\t"
        "v_mfma_f32_16x16x32_bf16 v[140:143], a[12:15], v[24:27], v[140:143]\n\t"
        "ds_read_b32 v54, v54\n\t"
        "v_lshl_or_b32 v92, v94, 4, v92\n\t"
        "v_mad_u32_u16 v51, v92, s48, v1\n\t"
        "v_mad_u32_u16 v55, v92, s48, v1 op_sel:[1,0,0,0]\n\t"
        "v_lshl_or_b32 v93, v95, 4, v93\n\t"
        "v_mfma_f32_16x16x32_bf16 v[132:135], a[4:7], v[24:27], 0\n\t"
        "ds_read_b128 v[24:27], v4 offset:18432\n\t"
        "v_mad_u32_u16 v59, v93, s48, v1\n\t"
        "v_mad_u32_u16 v63, v93, s48, v1 op_sel:[1,0,0,0]\n\t"
        "v_mfma_f32_16x16x32_bf16 v[132:135], a[0:3], v[44:47], v[132:135]\n\t"
        "buffer_load_format_d16_xyzw v[68:69], v10, s[8:11], s34 offen\n\t"
        "ds_read_b128 v[44:47], v4 offset:15360\n\t"
        "ds_read_b32 v58, v58\n\t"
        "v_mfma_f32_16x16x32_bf16 v[160:163], a[0:3], v[20:23], 0\n\t"
        "ds_read_b32 v62, v62\n\t"
        "ds_read_b32 v51, v51\n\t"
        "ds_read_b32 v55, v55\n\t"
        "v_mfma_f32_16x16x32_bf16 v[160:163], a[4:7], v[32:35], v[160:163]\n\t"
        "ds_read_b32 v59, v59\n\t"
        "ds_read_b32 v63, v63\n\t"
        "v_mfma_f32_16x16x32_bf16 v[168:171], a[12:15], v[32:35], 0\n\t"
        "buffer_load_format_d16_xyzw v[70:71], v10, s[8:11], s35 offen\n\t"
        "v_mfma_f32_16x16x32_bf16 v[168:171], a[8:11], v[20:23], v[168:171]\n\t"
        "v_mfma_f32_16x16x32_bf16 v[176:179], a[16:19], v[20:23], 0\n\t"
        "v_mfma_f32_16x16x32_bf16 v[176:179], a[20:23], v[32:35], v[176:179]\n\t"
        "v_mfma_f32_16x16x32_bf16 v[184:187], a[28:31], v[32:35], 0\n\t"
        "buffer_load_format_d16_xyzw v[72:73], v10, s[8:11], s36 offen\n\t"
        "ds_read_b128 v[32:35], v4 offset:19456\n\t"
        "v_mfma_f32_16x16x32_bf16 v[184:187], a[24:27], v[20:23], v[184:187]\n\t"
        "v_mfma_f32_16x16x32_bf16 v[188:191], a[24:27], v[28:31], 0\n\t"
        "v_mfma_f32_16x16x32_bf16 v[188:191], a[28:31], v[40:43], v[188:191]\n\t"
        "buffer_load_format_d16_xyzw v[74:75], v10, s[8:11], s37 offen\n\t"
        "s_waitcnt vmcnt(11) lgkmcnt(1)\n\t"
        "v_mfma_f32_16x16x32_bf16 v[180:183], a[20:23], v[40:43], 0\n\t"
        "buffer_load_format_d16_xyzw v[76:77], v10, s[8:11], s38 offen\n\t"
        "ds_read_b128 a[32:35], v7 offset:8192\n\t"
        "ds_read_b128 a[36:39], v8 offset:8192\n\t"
        "v_mfma_f32_16x16x32_bf16 v[180:183], a[16:19], v[28:31], v[180:183]\n\t"
        "ds_write_b128 v2, v[48:51] offset:16384\n\t"
        "v_mfma_f32_16x16x32_bf16 v[172:175], a[8:11], v[28:31], 0\n\t"
        "buffer_load_format_d16_xyzw v[78:79], v10, s[8:11], s39 offen\n\t"
        "v_mfma_f32_16x16x32_bf16 v[172:175], a[12:15], v[40:43], v[172:175]\n\t"
        "ds_read_b128 a[40:43], v7 offset:10240\n\t"
        "v_lshl_add_u32 v247, s55, 7, v248\n\t"
        "s_add_u32 m0, s53, 0\n\t"
        "ds_read_b128 a[44:47], v8 offset:10240\n\t"
        "v_mfma_f32_16x16x32_bf16 v[164:167], a[4:7], v[40:43], 0\n\t"
        "ds_write_b128 v2, v[52:55] offset:17408\n\t"
        "v_mfma_f32_16x16x32_bf16 v[164:167], a[0:3], v[28:31], v[164:167]\n\t"
        "buffer_load_dwordx4 v247, s[4:7], s40 offen lds\n\t"
        "v_mfma_f32_16x16x32_bf16 v[192:195], a[0:3], v[36:39], 0\n\t"
        "v_mfma_f32_16x16x32_bf16 v[192:195], a[4:7], v[24:27], v[192:195]\n\t"
        "ds_write_b128 v2, v[56:59] offset:18432\n\t"
        "v_mfma_f32_16x16x32_bf16 v[200:203], a[12:15], v[24:27], 0\n\t"
        "buffer_load_dwordx4 v247, s[4:7], s41 offen offset:1024 lds\n\t"
        "v_mfma_f32_16x16x32_bf16 v[200:203], a[8:11], v[36:39], v[200:203]\n\t"
        "buffer_load_dwordx4 v247, s[4:7], s42 offen offset:2048 lds\n\t"
        "v_mfma_f32_16x16x32_bf16 v[208:211], a[16:19], v[36:39], 0\n\t"
        "ds_write_b128 v2, v[60:63] offset:19456\n\t"
        "v_mfma_f32_16x16x32_bf16 v[208:211], a[20:23], v[24:27], v[208:211]\n\t"
        "s_waitcnt vmcnt(11) lgkmcnt(2)\n\t"
        "v_mfma_f32_16x16x32_bf16 v[216:219], a[28:31], v[24:27], 0\n\t"
        "buffer_load_dwordx4 v247, s[4:7], s43 offen offset:3072 lds\n\t"
        "ds_read_b128 a[48:51], v7 offset:12288\n\t"
        "s_cmp_eq_u32 s52, 2\n\t"
        "s_cselect_b32 s78, s10, 0\n\t"
        "v_mfma_f32_16x16x32_bf16 v[216:219], a[24:27], v[36:39], v[216:219]\n\t"
        "ds_read_b128 a[52:55], v8 offset:12288\n\t"
        "ds_read_b128 a[56:59], v7 offset:14336\n\t"
        "ds_read_b128 a[60:63], v8 offset:14336\n\t"
        "v_mfma_f32_16x16x32_bf16 v[220:223], a[24:27], v[44:47], 0\n\t"
        "buffer_load_dword v250, v245, s[76:79], 0 offen\n\t"
        "s_add_u32 m0, s53, 4096\n\t"
        "v_mfma_f32_16x16x32_bf16 v[220:223], a[28:31], v[32:35], v[220:223]\n\t"
        "s_waitcnt vmcnt(11) lgkmcnt(4)\n\t"
        "s_barrier\n\t"
        "v_mfma_f32_16x16x32_bf16 v[212:215], a[20:23], v[32:35], 0\n\t"
        "ds_read_b128 v[20:23], v4 offset:20480\n\t"
        "ds_read_b128 v[40:43], v4 offset:24576\n\t"
        "ds_read_b128 v[28:31], v4 offset:21504\n\t"
        "v_mfma_f32_16x16x32_bf16 v[212:215], a[16:19], v[44:47], v[212:215]\n\t"
        "buffer_load_dwordx4 v247, s[4:7], s44 offen lds\n\t"
        "ds_read_b128 v[24:27], v4 offset:25600\n\t"
        "v_mfma_f32_16x16x32_bf16 v[204:207], a[8:11], v[44:47], 0\n\t"
        "ds_read_b128 v[36:39], v4 offset:22528\n\t"
        "v_mfma_f32_16x16x32_bf16 v[204:207], a[12:15], v[32:35], v[204:207]\n\t"
        "buffer_load_dwordx4 v247, s[4:7], s45 offen offset:1024 lds\n\t"
        "s_waitcnt vmcnt(11) lgkmcnt(5)\n\t"
        "v_lshl_or_b32 v64, v66, 4, v64\n\t"
        "v_mad_u32_u16 v48, v64, s48, v1\n\t"
        "v_mfma_f32_16x16x32_bf16 v[196:199], a[4:7], v[32:35], 0\n\t"
        "ds_read_b128 v[32:35], v4 offset:26624\n\t"
        "v_mad_u32_u16 v52, v64, s48, v1 op_sel:[1,0,0,0]\n\t"
        "v_lshl_or_b32 v65, v67, 4, v65\n\t"
        "v_mad_u32_u16 v56, v65, s48, v1\n\t"
        "v_mad_u32_u16 v60, v65, s48, v1 op_sel:[1,0,0,0]\n\t"
        "v_mfma_f32_16x16x32_bf16 v[196:199], a[0:3], v[44:47], v[196:199]\n\t"
        "ds_read_b128 v[44:47], v4 offset:23552\n\t"
        "v_lshl_or_b32 v68, v70, 4, v68\n\t"
        "v_mad_u32_u16 v49, v68, s48, v1\n\t"
        "v_mad_u32_u16 v53, v68, s48, v1 op_sel:[1,0,0,0]\n\t"
        "s_waitcnt lgkmcnt(4)\n\t"
        "v_mfma_f32_16x16x32_bf16 v[96:99], a[32:35], v[20:23], v[96:99]\n\t"
        "buffer_load_dwordx4 v247, s[4:7], s46 offen offset:2048 lds\n\t"
        "v_mfma_f32_16x16x32_bf16 v[96:99], a[36:39], v[40:43], v[96:99]\n\t"
        "ds_read_b32 v48, v48\n\t"
        "v_lshl_or_b32 v69, v71, 4, v69\n\t"
        "v_mfma_f32_16x16x32_bf16 v[104:107], a[44:47], v[40:43], v[104:107]\n\t"
        "ds_read_b32 v52, v52\n\t"
        "v_mad_u32_u16 v57, v69, s48, v1\n\t"
        "v_mfma_f32_16x16x32_bf16 v[104:107], a[40:43], v[20:23], v[104:107]\n\t"
        "buffer_load_dwordx4 v247, s[4:7], s47 offen offset:3072 lds\n\t"
        "v_mfma_f32_16x16x32_bf16 v[112:115], a[48:51], v[20:23], v[112:115]\n\t"
        "ds_read_b32 v56, v56\n\t"
        "s_bfe_u32 s55, s72, 0x4000c\n\t"
        "v_mfma_f32_16x16x32_bf16 v[112:115], a[52:55], v[40:43], v[112:115]\n\t"
        "ds_read_b32 v60, v60\n\t"
        "s_mul_i32 s56, s55, 1179648\n\t"
        "v_mfma_f32_16x16x32_bf16 v[120:123], a[60:63], v[40:43], v[120:123]\n\t"
        "ds_read_b128 v[40:43], v4 offset:27648\n\t"
        "v_add_u32 v10, s56, v9\n\t"
        "v_mfma_f32_16x16x32_bf16 v[120:123], a[56:59], v[20:23], v[120:123]\n\t"
        "buffer_load_format_d16_xyzw v[80:81], v10, s[8:11], 0 offen\n\t"
        "s_waitcnt vmcnt(11) lgkmcnt(5)\n\t"
        "v_mfma_f32_16x16x32_bf16 v[124:127], a[56:59], v[28:31], v[124:127]\n\t"
        "ds_read_b128 v[20:23], v4 offset:28672\n\t"
        "v_mad_u32_u16 v61, v69, s48, v1 op_sel:[1,0,0,0]\n\t"
        "v_mfma_f32_16x16x32_bf16 v[124:127], a[60:63], v[24:27], v[124:127]\n\t"
        "ds_read_b32 v49, v49\n\t"
        "v_mfma_f32_16x16x32_bf16 v[116:119], a[52:55], v[24:27], v[116:119]\n\t"
        "ds_read_b32 v53, v53\n\t"
        "v_mfma_f32_16x16x32_bf16 v[116:119], a[48:51], v[28:31], v[116:119]\n\t"
        "buffer_load_format_d16_xyzw v[82:83], v10, s[8:11], s33 offen\n\t"
        "v_mfma_f32_16x16x32_bf16 v[108:111], a[40:43], v[28:31], v[108:111]\n\t"
        "ds_read_b32 v57, v57\n\t"
        "s_waitcnt vmcnt(11) lgkmcnt(5)\n\t"
        ".Lp4_%=:\n\t"
        "s_cbranch_execz .Lret0_%=\n\t"
        "v_mfma_f32_16x16x32_bf16 v[108:111], a[44:47], v[24:27], v[108:111]\n\t"
        "ds_read_b32 v61, v61\n\t"
        "v_lshl_or_b32 v72, v74, 4, v72\n\t"
        "v_mfma_f32_16x16x32_bf16 v[100:103], a[36:39], v[24:27], v[100:103]\n\t"
        "ds_read_b128 v[24:27], v4 offset:32768\n\t"
        "v_mad_u32_u16 v50, v72, s48, v1\n\t"
        "v_mfma_f32_16x16x32_bf16 v[100:103], a[32:35], v[28:31], v[100:103]\n\t"
        "buffer_load_format_d16_xyzw v[84:85], v10, s[8:11], s34 offen\n\t"
        "v_mfma_f32_16x16x32_bf16 v[128:131], a[32:35], v[36:39], v[128:131]\n\t"
        "ds_read_b128 v[28:31], v4 offset:29696\n\t"
        "v_mad_u32_u16 v54, v72, s48, v1 op_sel:[1,0,0,0]\n\t"
        "v_mfma_f32_16x16x32_bf16 v[128:131], a[36:39], v[32:35], v[128:131]\n\t"
        "ds_read_b32 v50, v50\n\t"
        "v_lshl_or_b32 v73, v75, 4, v73\n\t"
        "v_mfma_f32_16x16x32_bf16 v[136:139], a[44:47], v[32:35], v[136:139]\n\t"
        "ds_read_b32 v54, v54\n\t"
        "v_mad_u32_u16 v58, v73, s48, v1\n\t"
        "v_mfma_f32_16x16x32_bf16 v[136:139], a[40:43], v[36:39], v[136:139]\n\t"
        "buffer_load_format_d16_xyzw v[86:87], v10, s[8:11], s35 offen\n\t"
        "v_mfma_f32_16x16x32_bf16 v[144:147], a[48:51], v[36:39], v[144:147]\n\t"
        "ds_read_b32 v58, v58\n\t"
        "v_mad_u32_u16 v62, v73, s48, v1 op_sel:[1,0,0,0]\n\t"
        "v_mfma_f32_16x16x32_bf16 v[144:147], a[52:55], v[32:35], v[144:147]\n\t"
        "ds_read_b32 v62, v62\n\t"
        "v_lshl_or_b32 v76, v78, 4, v76\n\t"
        "v_mfma_f32_16x16x32_bf16 v[152:155], a[60:63], v[32:35], v[152:155]\n\t"
        "ds_read_b128 v[32:35], v4 offset:33792\n\t"
        "v_mad_u32_u16 v51, v76, s48, v1\n\t"
        "v_mfma_f32_16x16x32_bf16 v[152:155], a[56:59], v[36:39], v[152:155]\n\t"
        "ds_read_b128 v[36:39], v4 offset:30720\n\t"
        "s_waitcnt vmcnt(11) lgkmcnt(7)\n\t"
        "v_mfma_f32_16x16x32_bf16 v[156:159], a[56:59], v[44:47], v[156:159]\n\t"
        "buffer_load_format_d16_xyzw v[88:89], v10, s[8:11], s36 offen\n\t"
        "v_mfma_f32_16x16x32_bf16 v[156:159], a[60:63], v[40:43], v[156:159]\n\t"
        "ds_read_b32 v51, v51\n\t"
        "v_mad_u32_u16 v55, v76, s48, v1 op_sel:[1,0,0,0]\n\t"
        "v_mfma_f32_16x16x32_bf16 v[148:151], a[52:55], v[40:43], v[148:151]\n\t"
        "ds_read_b32 v55, v55\n\t"
        "v_lshl_or_b32 v77, v79, 4, v77\n\t"
        "v_mfma_f32_16x16x32_bf16 v[148:151], a[48:51], v[44:47], v[148:151]\n\t"
        "v_mad_u32_u16 v59, v77, s48, v1\n\t"
        "s_waitcnt vmcnt(10) lgkmcnt(5)\n\t"
        "v_mfma_f32_16x16x32_bf16 v[140:143], a[40:43], v[44:47], v[140:143]\n\t"
        "buffer_load_format_d16_xyzw v[90:91], v10, s[8:11], s37 offen\n\t"
        "v_mfma_f32_16x16x32_bf16 v[140:143], a[44:47], v[40:43], v[140:143]\n\t"
        "buffer_load_format_d16_xyzw v[92:93], v10, s[8:11], s38 offen\n\t"
        "v_mfma_f32_16x16x32_bf16 v[132:135], a[36:39], v[40:43], v[132:135]\n\t"
        "ds_read_b128 v[40:43], v4 offset:34816\n\t"
        "v_mad_u32_u16 v63, v77, s48, v1 op_sel:[1,0,0,0]\n\t"
        "v_mfma_f32_16x16x32_bf16 v[132:135], a[32:35], v[44:47], v[132:135]\n\t"
        "ds_read_b128 v[44:47], v4 offset:31744\n\t"
        "v_mfma_f32_16x16x32_bf16 v[160:163], a[32:35], v[20:23], v[160:163]\n\t"
        "ds_read_b32 v59, v59\n\t"
        "v_mfma_f32_16x16x32_bf16 v[160:163], a[36:39], v[24:27], v[160:163]\n\t"
        "buffer_load_format_d16_xyzw v[94:95], v10, s[8:11], s39 offen\n\t"
        "v_mfma_f32_16x16x32_bf16 v[168:171], a[44:47], v[24:27], v[168:171]\n\t"
        "ds_read_b32 v63, v63\n\t"
        "v_lshl_add_u32 v247, s55, 7, v248\n\t"
        "v_mfma_f32_16x16x32_bf16 v[168:171], a[40:43], v[20:23], v[168:171]\n\t"
        "ds_read_b128 a[64:67], v7\n\t"
        "s_add_u32 m0, s53, 8192\n\t"
        "v_mfma_f32_16x16x32_bf16 v[176:179], a[48:51], v[20:23], v[176:179]\n\t"
        "ds_read_b128 a[68:71], v8\n\t"
        "s_waitcnt vmcnt(11) lgkmcnt(6)\n\t"
        "v_mfma_f32_16x16x32_bf16 v[176:179], a[52:55], v[24:27], v[176:179]\n\t"
        "buffer_load_dwordx4 v247, s[4:7], s40 offen lds\n\t"
        "v_mfma_f32_16x16x32_bf16 v[184:187], a[60:63], v[24:27], v[184:187]\n\t"
        "ds_read_b128 v[24:27], v4 offset:35840\n\t"
        "v_mfma_f32_16x16x32_bf16 v[184:187], a[56:59], v[20:23], v[184:187]\n\t"
        "ds_write_b128 v2, v[48:51] offset:32768\n\t"
        "v_mfma_f32_16x16x32_bf16 v[188:191], a[56:59], v[28:31], v[188:191]\n\t"
        "v_mfma_f32_16x16x32_bf16 v[188:191], a[60:63], v[32:35], v[188:191]\n\t"
        "buffer_load_dwordx4 v247, s[4:7], s41 offen offset:1024 lds\n\t"
        "v_mfma_f32_16x16x32_bf16 v[180:183], a[52:55], v[32:35], v[180:183]\n\t"
        "ds_write_b128 v2, v[52:55] offset:33792\n\t"
        "v_mfma_f32_16x16x32_bf16 v[180:183], a[48:51], v[28:31], v[180:183]\n\t"
        "v_mfma_f32_16x16x32_bf16 v[172:175], a[40:43], v[28:31], v[172:175]\n\t"
        "buffer_load_dwordx4 v247, s[4:7], s42 offen offset:2048 lds\n\t"
        "v_mfma_f32_16x16x32_bf16 v[172:175], a[44:47], v[32:35], v[172:175]\n\t"
        "ds_read_b128 a[72:75], v7 offset:2048\n\t"
        "s_waitcnt vmcnt(11) lgkmcnt(3)\n\t"
        "v_mfma_f32_16x16x32_bf16 v[164:167], a[36:39], v[32:35], v[164:167]\n\t"
        "ds_write_b128 v2, v[56:59] offset:34816\n\t"
        "v_mfma_f32_16x16x32_bf16 v[164:167], a[32:35], v[28:31], v[164:167]\n\t"
        "buffer_load_dwordx4 v247, s[4:7], s43 offen offset:3072 lds\n\t"
        "v_mfma_f32_16x16x32_bf16 v[192:195], a[32:35], v[36:39], v[192:195]\n\t"
        "s_cmp_eq_u32 s52, 3\n\t"
        "s_cselect_b32 s78, s10, 0\n\t"
        "v_mfma_f32_16x16x32_bf16 v[192:195], a[36:39], v[40:43], v[192:195]\n\t"
        "ds_write_b128 v2, v[60:63] offset:35840\n\t"
        "v_mfma_f32_16x16x32_bf16 v[200:203], a[44:47], v[40:43], v[200:203]\n\t"
        "v_mfma_f32_16x16x32_bf16 v[200:203], a[40:43], v[36:39], v[200:203]\n\t"
        "buffer_load_dword v250, v245, s[76:79], 0 offen\n\t"
        "v_mfma_f32_16x16x32_bf16 v[208:211], a[48:51], v[36:39], v[208:211]\n\t"
        "ds_read_b128 a[76:79], v8 offset:2048\n\t"
        "s_add_u32 m0, s53, 12288\n\t"
        "v_mfma_f32_16x16x32_bf16 v[208:211], a[52:55], v[40:43], v[208:211]\n\t"
        "ds_read_b128 a[80:83], v7 offset:4096\n\t"
        "v_mfma_f32_16x16x32_bf16 v[216:219], a[60:63], v[40:43], v[216:219]\n\t"
        "ds_read_b128 a[84:87], v8 offset:4096\n\t"
        "v_mfma_f32_16x16x32_bf16 v[216:219], a[56:59], v[36:39], v[216:219]\n\t"
        "buffer_load_dwordx4 v247, s[4:7], s44 offen lds\n\t"
        "ds_read_b128 a[88:91], v7 offset:6144\n\t"
        "v_mfma_f32_16x16x32_bf16 v[220:223], a[56:59], v[44:47], v[220:223]\n\t"
        "ds_read_b128 a[92:95], v8 offset:6144\n\t"
        "v_mfma_f32_16x16x32_bf16 v[220:223], a[60:63], v[24:27], v[220:223]\n\t"
        "v_mfma_f32_16x16x32_bf16 v[212:215], a[52:55], v[24:27], v[212:215]\n\t"
        "s_waitcnt vmcnt(11) lgkmcnt(5)\n\t"
        "buffer_load_dwordx4 v247, s[4:7], s45 offen offset:1024 lds\n\t"
        "v_mfma_f32_16x16x32_bf16 v[212:215], a[48:51], v[44:47], v[212:215]\n\t"
        "s_barrier\n\t"
        "ds_read_b128 v[20:23], v4 offset:36864\n\t"
        "v_mfma_f32_16x16x32_bf16 v[204:207], a[40:43], v[44:47], v[204:207]\n\t"
        "ds_read_b128 v[32:35], v4 offset:40960\n\t"
        "s_waitcnt vmcnt(11) lgkmcnt(5)\n\t"
        "v_mfma_f32_16x16x32_bf16 v[204:207], a[44:47], v[24:27], v[204:207]\n\t"
        "ds_read_b128 v[28:31], v4 offset:37888\n\t"
        "v_lshl_or_b32 v80, v82, 4, v80\n\t"
        "v_mfma_f32_16x16x32_bf16 v[196:199], a[36:39], v[24:27], v[196:199]\n\t"
        "ds_read_b128 v[40:43], v4 offset:41984\n\t"
        "v_mad_u32_u16 v48, v80, s48, v1\n\t"
        "v_mfma_f32_16x16x32_bf16 v[196:199], a[32:35], v[44:47], v[196:199]\n\t"
        "buffer_load_dwordx4 v247, s[4:7], s46 offen offset:2048 lds\n\t"
        "ds_read_b128 v[36:39], v4 offset:38912\n\t"
        "v_mad_u32_u16 v52, v80, s48, v1 op_sel:[1,0,0,0]\n\t"
        "v_lshl_or_b32 v81, v83, 4, v81\n\t"
        "v_mad_u32_u16 v56, v81, s48, v1\n\t"
        "v_mad_u32_u16 v60, v81, s48, v1 op_sel:[1,0,0,0]\n\t"
        "v_lshl_or_b32 v84, v86, 4, v84\n\t"
        "v_mad_u32_u16 v49, v84, s48, v1\n\t"
        "v_mad_u32_u16 v53, v84, s48, v1 op_sel:[1,0,0,0]\n\t"
        "v_lshl_or_b32 v85, v87, 4, v85\n\t"
        "v_mad_u32_u16 v57, v85, s48, v1\n\t"
        "v_mad_u32_u16 v61, v85, s48, v1 op_sel:[1,0,0,0]\n\t"
        "buffer_load_dwordx4 v247, s[4:7], s47 offen offset:3072 lds\n\t"
        "ds_read_b128 v[24:27], v4 offset:43008\n\t"
        "s_waitcnt vmcnt(11) lgkmcnt(4)\n\t"
        "v_mfma_f32_16x16x32_bf16 v[96:99], a[64:67], v[20:23], v[96:99]\n\t"
        "ds_read_b128 v[44:47], v4 offset:39936\n\t"
        "s_bfe_u32 s55, s72, 0x40010\n\t"
        "v_mfma_f32_16x16x32_bf16 v[96:99], a[68:71], v[32:35], v[96:99]\n\t"
        "ds_read_b32 v48, v48\n\t"
        "s_mul_i32 s56, s55, 1179648\n\t"
        "v_mfma_f32_16x16x32_bf16 v[104:107], a[76:79], v[32:35], v[104:107]\n\t"
        "ds_read_b32 v52, v52\n\t"
        "v_add_u32 v10, s56, v9\n\t"
        "v_mfma_f32_16x16x32_bf16 v[104:107], a[72:75], v[20:23], v[104:107]\n\t"
        "buffer_load_format_d16_xyzw v[64:65], v10, s[8:11], 0 offen\n\t"
        "v_mfma_f32_16x16x32_bf16 v[112:115], a[80:83], v[20:23], v[112:115]\n\t"
        "ds_read_b32 v56, v56\n\t"
        "v_mfma_f32_16x16x32_bf16 v[112:115], a[84:87], v[32:35], v[112:115]\n\t"
        "ds_read_b32 v60, v60\n\t"
        "v_mfma_f32_16x16x32_bf16 v[120:123], a[92:95], v[32:35], v[120:123]\n\t"
        "ds_read_b128 v[32:35], v4 offset:44032\n\t"
        "s_waitcnt vmcnt(10) lgkmcnt(7)\n\t"
        "v_mfma_f32_16x16x32_bf16 v[120:123], a[88:91], v[20:23], v[120:123]\n\t"
        "buffer_load_format_d16_xyzw v[66:67], v10, s[8:11], s33 offen\n\t"
        "v_mfma_f32_16x16x32_bf16 v[124:127], a[88:91], v[28:31], v[124:127]\n\t"
        "ds_read_b128 v[20:23], v4 offset:45056\n\t"
        "v_lshl_or_b32 v88, v90, 4, v88\n\t"
        "v_mfma_f32_16x16x32_bf16 v[124:127], a[92:95], v[40:43], v[124:127]\n\t"
        "ds_read_b32 v49, v49\n\t"
        "v_mad_u32_u16 v50, v88, s48, v1\n\t"
        "v_mfma_f32_16x16x32_bf16 v[116:119], a[84:87], v[40:43], v[116:119]\n\t"
        "ds_read_b32 v53, v53\n\t"
        "v_mad_u32_u16 v54, v88, s48, v1 op_sel:[1,0,0,0]\n\t"
        "v_mfma_f32_16x16x32_bf16 v[116:119], a[80:83], v[28:31], v[116:119]\n\t"
        "buffer_load_format_d16_xyzw v[68:69], v10, s[8:11], s34 offen\n\t"
        "v_mfma_f32_16x16x32_bf16 v[108:111], a[72:75], v[28:31], v[108:111]\n\t"
        "ds_read_b32 v57, v57\n\t"
        "v_lshl_or_b32 v89, v91, 4, v89\n\t"
        "v_mfma_f32_16x16x32_bf16 v[108:111], a[76:79], v[40:43], v[108:111]\n\t"
        "ds_read_b32 v61, v61\n\t"
        "v_mad_u32_u16 v58, v89, s48, v1\n\t"
        "v_mfma_f32_16x16x32_bf16 v[100:103], a[68:71], v[40:43], v[100:103]\n\t"
        "ds_read_b128 v[40:43], v4 offset:49152\n\t"
        "v_mad_u32_u16 v62, v89, s48, v1 op_sel:[1,0,0,0]\n\t"
        "v_mfma_f32_16x16x32_bf16 v[100:103], a[64:67], v[28:31], v[100:103]\n\t"
        "buffer_load_format_d16_xyzw v[70:71], v10, s[8:11], s35 offen\n\t"
        "s_waitcnt vmcnt(11) lgkmcnt(5)\n\t"
        "v_mfma_f32_16x16x32_bf16 v[128:131], a[64:67], v[36:39], v[128:131]\n\t"
        "ds_read_b128 v[28:31], v4 offset:46080\n\t"
        "v_lshl_or_b32 v92, v94, 4, v92\n\t"
        "v_mfma_f32_16x16x32_bf16 v[128:131], a[68:71], v[24:27], v[128:131]\n\t"
        "ds_read_b32 v50, v50\n\t"
        "v_mad_u32_u16 v51, v92, s48, v1\n\t"
        "v_mfma_f32_16x16x32_bf16 v[136:139], a[76:79], v[24:27], v[136:139]\n\t"
        "ds_read_b32 v54, v54\n\t"
        "v_mad_u32_u16 v55, v92, s48, v1 op_sel:[1,0,0,0]\n\t"
        "v_mfma_f32_16x16x32_bf16 v[136:139], a[72:75], v[36:39], v[136:139]\n\t"
        "buffer_load_format_d16_xyzw v[72:73], v10, s[8:11], s36 offen\n\t"
        "v_mfma_f32_16x16x32_bf16 v[144:147], a[80:83], v[36:39], v[144:147]\n\t"
        "ds_read_b32 v58, v58\n\t"
        "v_lshl_or_b32 v93, v95, 4, v93\n\t"
        "v_mfma_f32_16x16x32_bf16 v[144:147], a[84:87], v[24:27], v[144:147]\n\t"
        "ds_read_b32 v62, v62\n\t"
        "v_mad_u32_u16 v59, v93, s48, v1\n\t"
        "v_mfma_f32_16x16x32_bf16 v[152:155], a[92:95], v[24:27], v[152:155]\n\t"
        "ds_read_b128 v[24:27], v4 offset:50176\n\t"
        "v_mad_u32_u16 v63, v93, s48, v1 op_sel:[1,0,0,0]\n\t"
        "v_mfma_f32_16x16x32_bf16 v[152:155], a[88:91], v[36:39], v[152:155]\n\t"
        "buffer_load_format_d16_xyzw v[74:75], v10, s[8:11], s37 offen\n\t"
        "v_mfma_f32_16x16x32_bf16 v[156:159], a[88:91], v[44:47], v[156:159]\n\t"
        "buffer_load_format_d16_xyzw v[76:77], v10, s[8:11], s38 offen\n\t"
        "v_mfma_f32_16x16x32_bf16 v[156:159], a[92:95], v[32:35], v[156:159]\n\t"
        "ds_read_b128 v[36:39], v4 offset:47104\n\t"
        "s_waitcnt vmcnt(11) lgkmcnt(4)\n\t"
        "v_mfma_f32_16x16x32_bf16 v[148:151], a[84:87], v[32:35], v[148:151]\n\t"
        "ds_read_b32 v51, v51\n\t"
        "v_mfma_f32_16x16x32_bf16 v[148:151], a[80:83], v[44:47], v[148:151]\n\t"
        "ds_read_b32 v55, v55\n\t"
        "v_mfma_f32_16x16x32_bf16 v[140:143], a[72:75], v[44:47], v[140:143]\n\t"
        "buffer_load_format_d16_xyzw v[78:79], v10, s[8:11], s39 offen\n\t"
        "v_mfma_f32_16x16x32_bf16 v[140:143], a[76:79], v[32:35], v[140:143]\n\t"
        "ds_read_b32 v59, v59\n\t"
        "v_lshl_add_u32 v247, s55, 7, v248\n\t"
        "v_mfma_f32_16x16x32_bf16 v[132:135], a[68:71], v[32:35], v[132:135]\n\t"
        "ds_read_b128 v[32:35], v4 offset:51200\n\t"
        "s_add_u32 m0, s53, 0\n\t"
        "v_mfma_f32_16x16x32_bf16 v[132:135], a[64:67], v[44:47], v[132:135]\n\t"
        "ds_read_b128 v[44:47], v4 offset:48128\n\t"
        "v_mfma_f32_16x16x32_bf16 v[160:163], a[64:67], v[20:23], v[160:163]\n\t"
        "buffer_load_dwordx4 v247, s[4:7], s40 offen lds\n\t"
        "v_mfma_f32_16x16x32_bf16 v[160:163], a[68:71], v[40:43], v[160:163]\n\t"
        "ds_read_b32 v63, v63\n\t"
        "s_waitcnt vmcnt(11) lgkmcnt(5)\n\t"
        "v_mfma_f32_16x16x32_bf16 v[168:171], a[76:79], v[40:43], v[168:171]\n\t"
        "ds_write_b128 v2, v[48:51]\n\t"
        "v_mfma_f32_16x16x32_bf16 v[168:171], a[72:75], v[20:23], v[168:171]\n\t"
        "v_mfma_f32_16x16x32_bf16 v[176:179], a[80:83], v[20:23], v[176:179]\n\t"
        "buffer_load_dwordx4 v247, s[4:7], s41 offen offset:1024 lds\n\t"
        "v_mfma_f32_16x16x32_bf16 v[176:179], a[84:87], v[40:43], v[176:179]\n\t"
        "ds_read_b128 a[96:99], v7 offset:8192\n\t"
        "s_waitcnt vmcnt(11) lgkmcnt(4)\n\t"
        "v_mfma_f32_16x16x32_bf16 v[184:187], a[92:95], v[40:43], v[184:187]\n\t"
        "ds_read_b128 v[40:43], v4 offset:52224\n\t"
        "v_mfma_f32_16x16x32_bf16 v[184:187], a[88:91], v[20:23], v[184:187]\n\t"
        "ds_write_b128 v2, v[52:55] offset:1024\n\t"
        "v_mfma_f32_16x16x32_bf16 v[188:191], a[88:91], v[28:31], v[188:191]\n\t"
        "buffer_load_dwordx4 v247, s[4:7], s42 offen offset:2048 lds\n\t"
        "v_mfma_f32_16x16x32_bf16 v[188:191], a[92:95], v[24:27], v[188:191]\n\t"
        "v_mfma_f32_16x16x32_bf16 v[180:183], a[84:87], v[24:27], v[180:183]\n\t"
        "ds_write_b128 v2, v[56:59] offset:2048\n\t"
        "v_mfma_f32_16x16x32_bf16 v[180:183], a[80:83], v[28:31], v[180:183]\n\t"
        "buffer_load_dwordx4 v247, s[4:7], s43 offen offset:3072 lds\n\t"
        "v_mfma_f32_16x16x32_bf16 v[172:175], a[72:75], v[28:31], v[172:175]\n\t"
        "s_cmp_eq_u32 s52, 4\n\t"
        "s_cselect_b32 s78, s10, 0\n\t"
        "v_mfma_f32_16x16x32_bf16 v[172:175], a[76:79], v[24:27], v[172:175]\n\t"
        "ds_read_b128 a[100:103], v8 offset:8192\n\t"
        "s_waitcnt vmcnt(11) lgkmcnt(3)\n\t"
        "v_mfma_f32_16x16x32_bf16 v[164:167], a[68:71], v[24:27], v[164:167]\n\t"
        "ds_write_b128 v2, v[60:63] offset:3072\n\t"
        "v_mfma_f32_16x16x32_bf16 v[164:167], a[64:67], v[28:31], v[164:167]\n\t"
        "buffer_load_dword v250, v245, s[76:79], 0 offen\n\t"
        "v_mfma_f32_16x16x32_bf16 v[192:195], a[64:67], v[36:39], v[192:195]\n\t"
        "s_add_u32 m0, s53, 4096\n\t"
        "v_mfma_f32_16x16x32_bf16 v[192:195], a[68:71], v[32:35], v[192:195]\n\t"
        "ds_read_b128 a[104:107], v7 offset:10240\n\t"
        "v_mfma_f32_16x16x32_bf16 v[200:203], a[76:79], v[32:35], v[200:203]\n\t"
        "buffer_load_dwordx4 v247, s[4:7], s44 offen lds\n\t"
        "v_mfma_f32_16x16x32_bf16 v[200:203], a[72:75], v[36:39], v[200:203]\n\t"
        "ds_read_b128 a[108:111], v8 offset:10240\n\t"
        "v_mfma_f32_16x16x32_bf16 v[208:211], a[80:83], v[36:39], v[208:211]\n\t"
        "ds_read_b128 a[112:115], v7 offset:12288\n\t"
        "v_mfma_f32_16x16x32_bf16 v[208:211], a[84:87], v[32:35], v[208:211]\n\t"
        "ds_read_b128 a[116:119], v8 offset:12288\n\t"
        "v_mfma_f32_16x16x32_bf16 v[216:219], a[92:95], v[32:35], v[216:219]\n\t"
        "buffer_load_dwordx4 v247, s[4:7], s45 offen offset:1024 lds\n\t"
        "ds_read_b128 a[120:123], v7 offset:14336\n\t"
        "v_mfma_f32_16x16x32_bf16 v[216:219], a[88:91], v[36:39], v[216:219]\n\t"
        "ds_read_b128 a[124:127], v8 offset:14336\n\t"
        "s_waitcnt vmcnt(11) lgkmcnt(6)\n\t"
        "s_barrier\n\t"
        "v_mfma_f32_16x16x32_bf16 v[220:223], a[88:91], v[44:47], v[220:223]\n\t"
        "ds_read_b128 v[20:23], v4 offset:4096\n\t"
        "v_lshl_or_b32 v64, v66, 4, v64\n\t"
        "v_mfma_f32_16x16x32_bf16 v[220:223], a[92:95], v[40:43], v[220:223]\n\t"
        "ds_read_b128 v[24:27], v4 offset:8192\n\t"
        "v_mad_u32_u16 v48, v64, s48, v1\n\t"
        "v_mfma_f32_16x16x32_bf16 v[212:215], a[84:87], v[40:43], v[212:215]\n\t"
        "buffer_load_dwordx4 v247, s[4:7], s46 offen offset:2048 lds\n\t"
        "v_mfma_f32_16x16x32_bf16 v[212:215], a[80:83], v[44:47], v[212:215]\n\t"
        "ds_read_b128 v[28:31], v4 offset:5120\n\t"
        "v_mad_u32_u16 v52, v64, s48, v1 op_sel:[1,0,0,0]\n\t"
        "v_mfma_f32_16x16x32_bf16 v[204:207], a[72:75], v[44:47], v[204:207]\n\t"
        "ds_read_b128 v[32:35], v4 offset:9216\n\t"
        "v_lshl_or_b32 v65, v67, 4, v65\n\t"
        "v_mfma_f32_16x16x32_bf16 v[204:207], a[76:79], v[40:43], v[204:207]\n\t"
        "ds_read_b128 v[36:39], v4 offset:6144\n\t"
        "v_mad_u32_u16 v56, v65, s48, v1\n\t"
        "v_mfma_f32_16x16x32_bf16 v[196:199], a[68:71], v[40:43], v[196:199]\n\t"
        "buffer_load_dwordx4 v247, s[4:7], s47 offen offset:3072 lds\n\t"
        "v_mfma_f32_16x16x32_bf16 v[196:199], a[64:67], v[44:47], v[196:199]\n\t"
        "ds_read_b128 v[40:43], v4 offset:10240\n\t"
        "s_bfe_u32 s55, s72, 0x40014\n\t"
        "ds_read_b128 v[44:47], v4 offset:7168\n\t"
        "s_mul_i32 s56, s55, 1179648\n\t"
        "ds_read_b32 v48, v48\n\t"
        "s_waitcnt vmcnt(11) lgkmcnt(6)\n\t"
        "v_mfma_f32_16x16x32_bf16 v[96:99], a[96:99], v[20:23], v[96:99]\n\t"
        "ds_read_b32 v52, v52\n\t"
        "v_add_u32 v10, s56, v9\n\t"
        "v_mfma_f32_16x16x32_bf16 v[96:99], a[100:103], v[24:27], v[96:99]\n\t"
        "buffer_load_format_d16_xyzw v[80:81], v10, s[8:11], 0 offen\n\t"
        "v_mfma_f32_16x16x32_bf16 v[104:107], a[108:111], v[24:27], v[104:107]\n\t"
        "ds_read_b32 v56, v56\n\t"
        "v_mad_u32_u16 v60, v65, s48, v1 op_sel:[1,0,0,0]\n\t"
        "v_mfma_f32_16x16x32_bf16 v[104:107], a[104:107], v[20:23], v[104:107]\n\t"
        "ds_read_b32 v60, v60\n\t"
        "v_lshl_or_b32 v68, v70, 4, v68\n\t"
        "v_mfma_f32_16x16x32_bf16 v[112:115], a[112:115], v[20:23], v[112:115]\n\t"
        "v_mad_u32_u16 v49, v68, s48, v1\n\t"
        "v_mad_u32_u16 v53, v68, s48, v1 op_sel:[1,0,0,0]\n\t"
        "v_mfma_f32_16x16x32_bf16 v[112:115], a[116:119], v[24:27], v[112:115]\n\t"
        "buffer_load_format_d16_xyzw v[82:83], v10, s[8:11], s33 offen\n\t"
        "v_mfma_f32_16x16x32_bf16 v[120:123], a[124:127], v[24:27], v[120:123]\n\t"
        "ds_read_b128 v[24:27], v4 offset:11264\n\t"
        "v_lshl_or_b32 v69, v71, 4, v69\n\t"
        "v_mfma_f32_16x16x32_bf16 v[120:123], a[120:123], v[20:23], v[120:123]\n\t"
        "ds_read_b128 v[20:23], v4 offset:12288\n\t"
        "s_waitcnt vmcnt(11) lgkmcnt(5)\n\t"
        "v_mfma_f32_16x16x32_bf16 v[124:127], a[120:123], v[28:31], v[124:127]\n\t"
        "ds_read_b32 v49, v49\n\t"
        "v_mad_u32_u16 v57, v69, s48, v1\n\t"
        "v_mfma_f32_16x16x32_bf16 v[124:127], a[124:127], v[32:35], v[124:127]\n\t"
        "buffer_load_format_d16_xyzw v[84:85], v10, s[8:11], s34 offen\n\t"
        "v_mfma_f32_16x16x32_bf16 v[116:119], a[116:119], v[32:35], v[116:119]\n\t"
        "ds_read_b32 v53, v53\n\t"
        "v_mad_u32_u16 v61, v69, s48, v1 op_sel:[1,0,0,0]\n\t"
        "v_mfma_f32_16x16x32_bf16 v[116:119], a[112:115], v[28:31], v[116:119]\n\t"
        "ds_read_b32 v57, v57\n\t"
        "v_lshl_or_b32 v72, v74, 4, v72\n\t"
        "v_mfma_f32_16x16x32_bf16 v[108:111], a[104:107], v[28:31], v[108:111]\n\t"
        "ds_read_b32 v61, v61\n\t"
        "v_mad_u32_u16 v50, v72, s48, v1\n\t"
        "v_mfma_f32_16x16x32_bf16 v[108:111], a[108:111], v[32:35], v[108:111]\n\t"
        "buffer_load_format_d16_xyzw v[86:87], v10, s[8:11], s35 offen\n\t"
        "v_mfma_f32_16x16x32_bf16 v[100:103], a[100:103], v[32:35], v[100:103]\n\t"
        "ds_read_b128 v[32:35], v4 offset:16384\n\t"
        "v_mad_u32_u16 v54, v72, s48, v1 op_sel:[1,0,0,0]\n\t"
        "v_mfma_f32_16x16x32_bf16 v[100:103], a[96:99], v[28:31], v[100:103]\n\t"
        "ds_read_b128 v[28:31], v4 offset:13312\n\t"
        "v_lshl_or_b32 v73, v75, 4, v73\n\t"
        "v_mfma_f32_16x16x32_bf16 v[128:131], a[96:99], v[36:39], v[128:131]\n\t"
        "ds_read_b32 v50, v50\n\t"
        "v_mad_u32_u16 v58, v73, s48, v1\n\t"
        "v_mfma_f32_16x16x32_bf16 v[128:131], a[100:103], v[40:43], v[128:131]\n\t"
        "ds_read_b32 v54, v54\n\t"
        "v_mad_u32_u16 v62, v73, s48, v1 op_sel:[1,0,0,0]\n\t"
        "v_mfma_f32_16x16x32_bf16 v[136:139], a[108:111], v[40:43], v[136:139]\n\t"
        "buffer_load_format_d16_xyzw v[88:89], v10, s[8:11], s36 offen\n\t"
        "v_mfma_f32_16x16x32_bf16 v[136:139], a[104:107], v[36:39], v[136:139]\n\t"
        "ds_read_b32 v58, v58\n\t"
        "v_lshl_or_b32 v76, v78, 4, v76\n\t"
        "v_mfma_f32_16x16x32_bf16 v[144:147], a[112:115], v[36:39], v[144:147]\n\t"
        "ds_read_b32 v62, v62\n\t"
        "v_mad_u32_u16 v51, v76, s48, v1\n\t"
        "v_mfma_f32_16x16x32_bf16 v[144:147], a[116:119], v[40:43], v[144:147]\n\t"
        "ds_read_b32 v51, v51\n\t"
        "s_waitcnt vmcnt(10) lgkmcnt(7)\n\t"
        "v_mfma_f32_16x16x32_bf16 v[152:155], a[124:127], v[40:43], v[152:155]\n\t"
        "buffer_load_format_d16_xyzw v[90:91], v10, s[8:11], s37 offen\n\t"
        "v_mfma_f32_16x16x32_bf16 v[152:155], a[120:123], v[36:39], v[152:155]\n\t"
        "buffer_load_format_d16_xyzw v[92:93], v10, s[8:11], s38 offen\n\t"
        "v_mfma_f32_16x16x32_bf16 v[156:159], a[120:123], v[44:47], v[156:159]\n\t"
        "ds_read_b128 v[40:43], v4 offset:17408\n\t"
        "v_mad_u32_u16 v55, v76, s48, v1 op_sel:[1,0,0,0]\n\t"
        "v_mfma_f32_16x16x32_bf16 v[156:159], a[124:127], v[24:27], v[156:159]\n\t"
        "ds_read_b128 v[36:39], v4 offset:14336\n\t"
        "v_lshl_or_b32 v77, v79, 4, v77\n\t"
        "v_mfma_f32_16x16x32_bf16 v[148:151], a[116:119], v[24:27], v[148:151]\n\t"
        "ds_read_b32 v55, v55\n\t"
        "v_mad_u32_u16 v59, v77, s48, v1\n\t"
        "v_mfma_f32_16x16x32_bf16 v[148:151], a[112:115], v[44:47], v[148:151]\n\t"
        "buffer_load_format_d16_xyzw v[94:95], v10, s[8:11], s39 offen\n\t"
        "v_mfma_f32_16x16x32_bf16 v[140:143], a[104:107], v[44:47], v[140:143]\n\t"
        "ds_read_b32 v59, v59\n\t"
        "v_lshl_add_u32 v247, s55, 7, v248\n\t"
        "v_mfma_f32_16x16x32_bf16 v[140:143], a[108:111], v[24:27], v[140:143]\n\t"
        "ds_read_b128 a[128:131], v7\n\t"
        "s_add_u32 m0, s53, 8192\n\t"
        "v_mfma_f32_16x16x32_bf16 v[132:135], a[100:103], v[24:27], v[132:135]\n\t"
        "ds_read_b128 v[24:27], v4 offset:18432\n\t"
        "v_mad_u32_u16 v63, v77, s48, v1 op_sel:[1,0,0,0]\n\t"
        "v_mfma_f32_16x16x32_bf16 v[132:135], a[96:99], v[44:47], v[132:135]\n\t"
        "buffer_load_dwordx4 v247, s[4:7], s40 offen lds\n\t"
        "s_waitcnt vmcnt(11) lgkmcnt(5)\n\t"
        "v_mfma_f32_16x16x32_bf16 v[160:163], a[96:99], v[20:23], v[160:163]\n\t"
        "ds_read_b128 v[44:47], v4 offset:15360\n\t"
        "v_mfma_f32_16x16x32_bf16 v[160:163], a[100:103], v[32:35], v[160:163]\n\t"
        "ds_read_b32 v63, v63\n\t"
        "v_mfma_f32_16x16x32_bf16 v[168:171], a[108:111], v[32:35], v[168:171]\n\t"
        "ds_write_b128 v2, v[48:51] offset:16384\n\t"
        "v_mfma_f32_16x16x32_bf16 v[168:171], a[104:107], v[20:23], v[168:171]\n\t"
        "buffer_load_dwordx4 v247, s[4:7], s41 offen offset:1024 lds\n\t"
        "v_mfma_f32_16x16x32_bf16 v[176:179], a[112:115], v[20:23], v[176:179]\n\t"
        "s_waitcnt vmcnt(11) lgkmcnt(4)\n\t"
        "v_mfma_f32_16x16x32_bf16 v[176:179], a[116:119], v[32:35], v[176:179]\n\t"
        "ds_write_b128 v2, v[52:55] offset:17408\n\t"
        "v_mfma_f32_16x16x32_bf16 v[184:187], a[124:127], v[32:35], v[184:187]\n\t"
        "buffer_load_dwordx4 v247, s[4:7], s42 offen offset:2048 lds\n\t"
        "v_mfma_f32_16x16x32_bf16 v[184:187], a[120:123], v[20:23], v[184:187]\n\t"
        "v_mfma_f32_16x16x32_bf16 v[188:191], a[120:123], v[28:31], v[188:191]\n\t"
        "ds_read_b128 v[32:35], v4 offset:19456\n\t"
        "v_mfma_f32_16x16x32_bf16 v[188:191], a[124:127], v[40:43], v[188:191]\n\t"
        "ds_write_b128 v2, v[56:59] offset:18432\n\t"
        "v_mfma_f32_16x16x32_bf16 v[180:183], a[116:119], v[40:43], v[180:183]\n\t"
        "buffer_load_dwordx4 v247, s[4:7], s43 offen offset:3072 lds\n\t"
        "v_mfma_f32_16x16x32_bf16 v[180:183], a[112:115], v[28:31], v[180:183]\n\t"
        "s_cmp_eq_u32 s52, 5\n\t"
        "s_cselect_b32 s78, s10, 0\n\t"
        "v_mfma_f32_16x16x32_bf16 v[172:175], a[104:107], v[28:31], v[172:175]\n\t"
        "ds_read_b128 a[132:135], v8\n\t"
        "s_waitcnt vmcnt(11) lgkmcnt(4)\n\t"
        "v_mfma_f32_16x16x32_bf16 v[172:175], a[108:111], v[40:43], v[172:175]\n\t"
        "buffer_load_dword v250, v245, s[76:79], 0 offen\n\t"
        "v_mfma_f32_16x16x32_bf16 v[164:167], a[100:103], v[40:43], v[164:167]\n\t"
        "ds_write_b128 v2, v[60:63] offset:19456\n\t"
        "v_mfma_f32_16x16x32_bf16 v[164:167], a[96:99], v[28:31], v[164:167]\n\t"
        "s_add_u32 m0, s53, 12288\n\t"
        "v_mfma_f32_16x16x32_bf16 v[192:195], a[96:99], v[36:39], v[192:195]\n\t"
        "v_mfma_f32_16x16x32_bf16 v[192:195], a[100:103], v[24:27], v[192:195]\n\t"
        "buffer_load_dwordx4 v247, s[4:7], s44 offen lds\n\t"
        "v_mfma_f32_16x16x32_bf16 v[200:203], a[108:111], v[24:27], v[200:203]\n\t"
        "ds_read_b128 a[136:139], v7 offset:2048\n\t"
        "v_mfma_f32_16x16x32_bf16 v[200:203], a[104:107], v[36:39], v[200:203]\n\t"
        "ds_read_b128 a[140:143], v8 offset:2048\n\t"
        "v_mfma_f32_16x16x32_bf16 v[208:211], a[112:115], v[36:39], v[208:211]\n\t"
        "ds_read_b128 a[144:147], v7 offset:4096\n\t"
        "s_waitcnt vmcnt(10) lgkmcnt(4)\n\t"
        "v_mfma_f32_16x16x32_bf16 v[208:211], a[116:119], v[24:27], v[208:211]\n\t"
        "buffer_load_dwordx4 v247, s[4:7], s45 offen offset:1024 lds\n\t"
        "ds_read_b128 a[148:151], v8 offset:4096\n\t"
        "v_mfma_f32_16x16x32_bf16 v[216:219], a[124:127], v[24:27], v[216:219]\n\t"
        "ds_read_b128 a[152:155], v7 offset:6144\n\t"
        "v_lshl_or_b32 v80, v82, 4, v80\n\t"
        "s_waitcnt lgkmcnt(5)\n\t"
        "v_mfma_f32_16x16x32_bf16 v[216:219], a[120:123], v[36:39], v[216:219]\n\t"
        "ds_read_b128 a[156:159], v8 offset:6144\n\t"
        "s_barrier\n\t"
        "v_mfma_f32_16x16x32_bf16 v[220:223], a[120:123], v[44:47], v[220:223]\n\t"
        "buffer_load_dwordx4 v247, s[4:7], s46 offen offset:2048 lds\n\t"
        "v_mfma_f32_16x16x32_bf16 v[220:223], a[124:127], v[32:35], v[220:223]\n\t"
        "ds_read_b128 v[20:23], v4 offset:20480\n\t"
        "v_mad_u32_u16 v48, v80, s48, v1\n\t"
        "v_mfma_f32_16x16x32_bf16 v[212:215], a[116:119], v[32:35], v[212:215]\n\t"
        "ds_read_b128 v[40:43], v4 offset:24576\n\t"
        "v_mad_u32_u16 v52, v80, s48, v1 op_sel:[1,0,0,0]\n\t"
        "v_mfma_f32_16x16x32_bf16 v[212:215], a[112:115], v[44:47], v[212:215]\n\t"
        "ds_read_b128 v[28:31], v4 offset:21504\n\t"
        "v_lshl_or_b32 v81, v83, 4, v81\n\t"
        "v_mfma_f32_16x16x32_bf16 v[204:207], a[104:107], v[44:47], v[204:207]\n\t"
        "buffer_load_dwordx4 v247, s[4:7], s47 offen offset:3072 lds\n\t"
        "v_mfma_f32_16x16x32_bf16 v[204:207], a[108:111], v[32:35], v[204:207]\n\t"
        "ds_read_b128 v[24:27], v4 offset:25600\n\t"
        "v_mad_u32_u16 v56, v81, s48, v1\n\t"
        "v_mfma_f32_16x16x32_bf16 v[196:199], a[100:103], v[32:35], v[196:199]\n\t"
        "ds_read_b128 v[36:39], v4 offset:22528\n\t"
        "v_mad_u32_u16 v60, v81, s48, v1 op_sel:[1,0,0,0]\n\t"
        "v_mfma_f32_16x16x32_bf16 v[196:199], a[96:99], v[44:47], v[196:199]\n\t"
        "ds_read_b128 v[32:35], v4 offset:26624\n\t"
        "v_lshl_or_b32 v84, v86, 4, v84\n\t"
        "v_mad_u32_u16 v49, v84, s48, v1\n\t"
        "v_mad_u32_u16 v53, v84, s48, v1 op_sel:[1,0,0,0]\n\t"
        "v_lshl_or_b32 v85, v87, 4, v85\n\t"
        "v_mad_u32_u16 v57, v85, s48, v1\n\t"
        "v_mad_u32_u16 v61, v85, s48, v1 op_sel:[1,0,0,0]\n\t"
        "s_waitcnt vmcnt(11) lgkmcnt(4)\n\t"
        "v_mfma_f32_16x16x32_bf16 v[96:99], a[128:131], v[20:23], v[96:99]\n\t"
        "buffer_load_format_d16_xyzw v[224:225], v15, s[8:11], 0 offen\n\t"
        "v_mfma_f32_16x16x32_bf16 v[96:99], a[132:135], v[40:43], v[96:99]\n\t"
        "ds_read_b128 v[44:47], v4 offset:23552\n\t"
        "v_mfma_f32_16x16x32_bf16 v[104:107], a[140:143], v[40:43], v[104:107]\n\t"
        "ds_read_b32 v48, v48\n\t"
        "s_waitcnt vmcnt(10) lgkmcnt(5)\n\t"
        "v_mfma_f32_16x16x32_bf16 v[104:107], a[136:139], v[20:23], v[104:107]\n\t"
        "buffer_load_format_d16_xyzw v[226:227], v15, s[8:11], s33 offen\n\t"
        "v_mfma_f32_16x16x32_bf16 v[112:115], a[144:147], v[20:23], v[112:115]\n\t"
        "ds_read_b32 v52, v52\n\t"
        "s_bfe_u32 s55, s72, 0x40018\n\t"
        "v_mfma_f32_16x16x32_bf16 v[112:115], a[148:151], v[40:43], v[112:115]\n\t"
        "ds_read_b32 v56, v56\n\t"
        "s_mul_i32 s56, s55, 1179648\n\t"
        "v_mfma_f32_16x16x32_bf16 v[120:123], a[156:159], v[40:43], v[120:123]\n\t"
        "ds_read_b128 v[40:43], v4 offset:27648\n\t"
        "v_add_u32 v10, s56, v9\n\t"
        "v_mfma_f32_16x16x32_bf16 v[120:123], a[152:155], v[20:23], v[120:123]\n\t"
        "buffer_load_format_d16_xyzw v[64:65], v10, s[8:11], 0 offen\n\t"
        "s_waitcnt vmcnt(11) lgkmcnt(5)\n\t"
        "v_mfma_f32_16x16x32_bf16 v[124:127], a[152:155], v[28:31], v[124:127]\n\t"
        "ds_read_b128 v[20:23], v4 offset:28672\n\t"
        "v_lshl_or_b32 v88, v90, 4, v88\n\t"
        "v_mfma_f32_16x16x32_bf16 v[124:127], a[156:159], v[24:27], v[124:127]\n\t"
        "ds_read_b32 v60, v60\n\t"
        "v_mad_u32_u16 v50, v88, s48, v1\n\t"
        "v_mfma_f32_16x16x32_bf16 v[116:119], a[148:151], v[24:27], v[116:119]\n\t"
        "ds_read_b32 v49, v49\n\t"
        "v_mad_u32_u16 v54, v88, s48, v1 op_sel:[1,0,0,0]\n\t"
        "v_mfma_f32_16x16x32_bf16 v[116:119], a[144:147], v[28:31], v[116:119]\n\t"
        "buffer_load_format_d16_xyzw v[66:67], v10, s[8:11], s33 offen\n\t"
        "v_mfma_f32_16x16x32_bf16 v[108:111], a[136:139], v[28:31], v[108:111]\n\t"
        "ds_read_b32 v53, v53\n\t"
        "v_lshl_or_b32 v89, v91, 4, v89\n\t"
        "v_mfma_f32_16x16x32_bf16 v[108:111], a[140:143], v[24:27], v[108:111]\n\t"
        "ds_read_b32 v57, v57\n\t"
        "v_mad_u32_u16 v58, v89, s48, v1\n\t"
        "v_mfma_f32_16x16x32_bf16 v[100:103], a[132:135], v[24:27], v[100:103]\n\t"
        "ds_read_b128 v[24:27], v4 offset:32768\n\t"
        "v_mad_u32_u16 v62, v89, s48, v1 op_sel:[1,0,0,0]\n\t"
        "v_mfma_f32_16x16x32_bf16 v[100:103], a[128:131], v[28:31], v[100:103]\n\t"
        "buffer_load_format_d16_xyzw v[68:69], v10, s[8:11], s34 offen\n\t"
        "v_mfma_f32_16x16x32_bf16 v[128:131], a[128:131], v[36:39], v[128:131]\n\t"
        "ds_read_b128 v[28:31], v4 offset:29696\n\t"
        "v_lshl_or_b32 v92, v94, 4, v92\n\t"
        "v_mfma_f32_16x16x32_bf16 v[128:131], a[132:135], v[32:35], v[128:131]\n\t"
        "ds_read_b32 v61, v61\n\t"
        "v_mad_u32_u16 v51, v92, s48, v1\n\t"
        "v_mfma_f32_16x16x32_bf16 v[136:139], a[140:143], v[32:35], v[136:139]\n\t"
        "ds_read_b32 v50, v50\n\t"
        "v_mad_u32_u16 v55, v92, s48, v1 op_sel:[1,0,0,0]\n\t"
        "v_mfma_f32_16x16x32_bf16 v[136:139], a[136:139], v[36:39], v[136:139]\n\t"
        "buffer_load_format_d16_xyzw v[70:71], v10, s[8:11], s35 offen\n\t"
        "v_mfma_f32_16x16x32_bf16 v[144:147], a[144:147], v[36:39], v[144:147]\n\t"
        "buffer_load_format_d16_xyzw v[72:73], v10, s[8:11], s36 offen\n\t"
        "v_mfma_f32_16x16x32_bf16 v[144:147], a[148:151], v[32:35], v[144:147]\n\t"
        "ds_read_b32 v54, v54\n\t"
        "s_waitcnt vmcnt(11) lgkmcnt(5)\n\t"
        "v_mfma_f32_16x16x32_bf16 v[152:155], a[156:159], v[32:35], v[152:155]\n\t"
        "ds_read_b128 v[32:35], v4 offset:33792\n\t"
        "v_lshl_or_b32 v93, v95, 4, v93\n\t"
        ".Lp8_%=:\n\t"
        "s_cbranch_execz .Lret0_%=\n\t"
        "v_mfma_f32_16x16x32_bf16 v[152:155], a[152:155], v[36:39], v[152:155]\n\t"
        "ds_read_b128 v[36:39], v4 offset:30720\n\t"
        "v_mad_u32_u16 v59, v93, s48, v1\n\t"
        "v_mfma_f32_16x16x32_bf16 v[156:159], a[152:155], v[44:47], v[156:159]\n\t"
        "buffer_load_format_d16_xyzw v[74:75], v10, s[8:11], s37 offen\n\t"
        "v_mfma_f32_16x16x32_bf16 v[156:159], a[156:159], v[40:43], v[156:159]\n\t"
        "ds_read_b32 v58, v58\n\t"
        "v_mad_u32_u16 v63, v93, s48, v1 op_sel:[1,0,0,0]\n\t"
        "v_mfma_f32_16x16x32_bf16 v[148:151], a[148:151], v[40:43], v[148:151]\n\t"
        "ds_read_b32 v62, v62\n\t"
        "v_mfma_f32_16x16x32_bf16 v[148:151], a[144:147], v[44:47], v[148:151]\n\t"
        "ds_read_b32 v51, v51\n\t"
        "v_mfma_f32_16x16x32_bf16 v[140:143], a[136:139], v[44:47], v[140:143]\n\t"
        "buffer_load_format_d16_xyzw v[76:77], v10, s[8:11], s38 offen\n\t"
        "v_mfma_f32_16x16x32_bf16 v[140:143], a[140:143], v[40:43], v[140:143]\n\t"
        "ds_read_b32 v55, v55\n\t"
        "v_mfma_f32_16x16x32_bf16 v[132:135], a[132:135], v[40:43], v[132:135]\n\t"
        "ds_read_b128 v[40:43], v4 offset:34816\n\t"
        "v_mfma_f32_16x16x32_bf16 v[132:135], a[128:131], v[44:47], v[132:135]\n\t"
        "ds_read_b128 v[44:47], v4 offset:31744\n\t"
        "s_waitcnt vmcnt(11) lgkmcnt(6)\n\t"
        "v_mfma_f32_16x16x32_bf16 v[160:163], a[128:131], v[20:23], v[160:163]\n\t"
        "ds_read_b32 v59, v59\n\t"
        "v_mfma_f32_16x16x32_bf16 v[160:163], a[132:135], v[24:27], v[160:163]\n\t"
        "buffer_load_format_d16_xyzw v[78:79], v10, s[8:11], s39 offen\n\t"
        "v_mfma_f32_16x16x32_bf16 v[168:171], a[140:143], v[24:27], v[168:171]\n\t"
        "ds_read_b32 v63, v63\n\t"
        "v_lshl_add_u32 v247, s55, 7, v248\n\t"
        "v_mfma_f32_16x16x32_bf16 v[168:171], a[136:139], v[20:23], v[168:171]\n\t"
        "ds_read_b128 a[160:163], v7 offset:8192\n\t"
        "s_add_u32 m0, s53, 0\n\t"
        "v_mfma_f32_16x16x32_bf16 v[176:179], a[144:147], v[20:23], v[176:179]\n\t"
        "ds_read_b128 a[164:167], v8 offset:8192\n\t"
        "s_waitcnt vmcnt(11) lgkmcnt(6)\n\t"
        "v_mfma_f32_16x16x32_bf16 v[176:179], a[148:151], v[24:27], v[176:179]\n\t"
        "buffer_load_dwordx4 v247, s[4:7], s40 offen lds\n\t"
        "v_mfma_f32_16x16x32_bf16 v[184:187], a[156:159], v[24:27], v[184:187]\n\t"
        "ds_read_b128 v[24:27], v4 offset:35840\n\t"
        "v_mfma_f32_16x16x32_bf16 v[184:187], a[152:155], v[20:23], v[184:187]\n\t"
        "ds_write_b128 v2, v[48:51] offset:32768\n\t"
        "v_mfma_f32_16x16x32_bf16 v[188:191], a[152:155], v[28:31], v[188:191]\n\t"
        "buffer_load_dwordx4 v247, s[4:7], s41 offen offset:1024 lds\n\t"
        "v_mfma_f32_16x16x32_bf16 v[188:191], a[156:159], v[32:35], v[188:191]\n\t"
        "v_mfma_f32_16x16x32_bf16 v[180:183], a[148:151], v[32:35], v[180:183]\n\t"
        "ds_write_b128 v2, v[52:55] offset:33792\n\t"
        "v_mfma_f32_16x16x32_bf16 v[180:183], a[144:147], v[28:31], v[180:183]\n\t"
        "buffer_load_dwordx4 v247, s[4:7], s42 offen offset:2048 lds\n\t"
        "v_mfma_f32_16x16x32_bf16 v[172:175], a[136:139], v[28:31], v[172:175]\n\t"
        "s_waitcnt vmcnt(11) lgkmcnt(2)\n\t"
        "v_lshl_or_b32 v224, v226, 4, v224\n\t"
        "v_mfma_f32_16x16x32_bf16 v[172:175], a[140:143], v[32:35], v[172:175]\n\t"
        "ds_write_b128 v2, v[56:59] offset:34816\n\t"
        "v_mfma_f32_16x16x32_bf16 v[164:167], a[132:135], v[32:35], v[164:167]\n\t"
        "v_lshl_or_b32 v225, v227, 4, v225\n\t"
        "v_mad_u32_u16 v234, v224, s48, v1\n\t"
        "v_mfma_f32_16x16x32_bf16 v[164:167], a[128:131], v[28:31], v[164:167]\n\t"
        "buffer_load_dwordx4 v247, s[4:7], s43 offen offset:3072 lds\n\t"
        "v_mfma_f32_16x16x32_bf16 v[192:195], a[128:131], v[36:39], v[192:195]\n\t"
        "ds_write_b128 v2, v[60:63] offset:35840\n\t"
        "v_mfma_f32_16x16x32_bf16 v[192:195], a[132:135], v[40:43], v[192:195]\n\t"
        "s_cmp_eq_u32 s52, 6\n\t"
        "s_cselect_b32 s78, s10, 0\n\t"
        "v_mfma_f32_16x16x32_bf16 v[200:203], a[140:143], v[40:43], v[200:203]\n\t"
        "buffer_load_dword v250, v245, s[76:79], 0 offen\n\t"
        "v_mfma_f32_16x16x32_bf16 v[200:203], a[136:139], v[36:39], v[200:203]\n\t"
        "ds_read_b128 a[168:171], v7 offset:10240\n\t"
        "s_add_u32 m0, s53, 4096\n\t"
        "v_mfma_f32_16x16x32_bf16 v[208:211], a[144:147], v[36:39], v[208:211]\n\t"
        "ds_read_b128 a[172:175], v8 offset:10240\n\t"
        "v_mad_u32_u16 v235, v224, s48, v1 op_sel:[1,0,0,0]\n\t"
        "v_mfma_f32_16x16x32_bf16 v[208:211], a[148:151], v[40:43], v[208:211]\n\t"
        "ds_read_b128 a[176:179], v7 offset:12288\n\t"
        "v_mad_u32_u16 v236, v225, s48, v1\n\t"
        "v_mad_u32_u16 v237, v225, s48, v1 op_sel:[1,0,0,0]\n\t"
        "v_mfma_f32_16x16x32_bf16 v[216:219], a[156:159], v[40:43], v[216:219]\n\t"
        "buffer_load_dwordx4 v247, s[4:7], s44 offen lds\n\t"
        "ds_read_b128 a[180:183], v8 offset:12288\n\t"
        "v_mfma_f32_16x16x32_bf16 v[216:219], a[152:155], v[36:39], v[216:219]\n\t"
        "ds_read_b128 a[184:187], v7 offset:14336\n\t"
        "v_mfma_f32_16x16x32_bf16 v[220:223], a[152:155], v[44:47], v[220:223]\n\t"
        "ds_read_b128 a[188:191], v8 offset:14336\n\t"
        "s_waitcnt vmcnt(11) lgkmcnt(6)\n\t"
        "ds_read_b32 v234, v234\n\t"
        "v_mfma_f32_16x16x32_bf16 v[220:223], a[156:159], v[24:27], v[220:223]\n\t"
        "ds_read_b32 v235, v235\n\t"
        "buffer_load_dwordx4 v247, s[4:7], s45 offen offset:1024 lds\n\t"
        "v_mfma_f32_16x16x32_bf16 v[212:215], a[148:151], v[24:27], v[212:215]\n\t"
        "ds_read_b32 v236, v236\n\t"
        "s_barrier\n\t"
        "v_mfma_f32_16x16x32_bf16 v[212:215], a[144:147], v[44:47], v[212:215]\n\t"
        "ds_read_b128 v[20:23], v4 offset:36864\n\t"
        "s_waitcnt vmcnt(11) lgkmcnt(8)\n\t"
        "v_mfma_f32_16x16x32_bf16 v[204:207], a[136:139], v[44:47], v[204:207]\n\t"
        "ds_read_b128 v[32:35], v4 offset:40960\n\t"
        "v_lshl_or_b32 v64, v66, 4, v64\n\t"
        "v_mfma_f32_16x16x32_bf16 v[204:207], a[140:143], v[24:27], v[204:207]\n\t"
        "ds_read_b128 v[28:31], v4 offset:37888\n\t"
        "v_mad_u32_u16 v48, v64, s48, v1\n\t"
        "v_mfma_f32_16x16x32_bf16 v[196:199], a[132:135], v[24:27], v[196:199]\n\t"
        "buffer_load_dwordx4 v247, s[4:7], s46 offen offset:2048 lds\n\t"
        "v_mfma_f32_16x16x32_bf16 v[196:199], a[128:131], v[44:47], v[196:199]\n\t"
        "ds_read_b128 v[40:43], v4 offset:41984\n\t"
        "v_mad_u32_u16 v52, v64, s48, v1 op_sel:[1,0,0,0]\n\t"
        "v_lshl_or_b32 v65, v67, 4, v65\n\t"
        "v_mad_u32_u16 v56, v65, s48, v1\n\t"
        "v_mad_u32_u16 v60, v65, s48, v1 op_sel:[1,0,0,0]\n\t"
        "v_lshl_or_b32 v68, v70, 4, v68\n\t"
        "v_mad_u32_u16 v49, v68, s48, v1\n\t"
        "v_mad_u32_u16 v53, v68, s48, v1 op_sel:[1,0,0,0]\n\t"
        "v_lshl_or_b32 v69, v71, 4, v69\n\t"
        "v_mad_u32_u16 v57, v69, s48, v1\n\t"
        "v_mad_u32_u16 v61, v69, s48, v1 op_sel:[1,0,0,0]\n\t"
        "buffer_load_dwordx4 v247, s[4:7], s47 offen offset:3072 lds\n\t"
        "ds_read_b128 v[36:39], v4 offset:38912\n\t"
        "s_waitcnt vmcnt(11) lgkmcnt(3)\n\t"
        "v_mfma_f32_16x16x32_bf16 v[96:99], a[160:163], v[20:23], v[96:99]\n\t"
        "ds_read_b128 v[24:27], v4 offset:43008\n\t"
        "v_mfma_f32_16x16x32_bf16 v[96:99], a[164:167], v[32:35], v[96:99]\n\t"
        "ds_read_b128 v[44:47], v4 offset:39936\n\t"
        "v_mfma_f32_16x16x32_bf16 v[104:107], a[172:175], v[32:35], v[104:107]\n\t"
        "buffer_load_dwordx2 v[228:229], v17, s[16:19], 0 offen\n\t"
        "v_mfma_f32_16x16x32_bf16 v[104:107], a[168:171], v[20:23], v[104:107]\n\t"
        "ds_read_b32 v48, v48\n\t"
        "v_mfma_f32_16x16x32_bf16 v[112:115], a[176:179], v[20:23], v[112:115]\n\t"
        "ds_read_b32 v52, v52\n\t"
        "v_mfma_f32_16x16x32_bf16 v[112:115], a[180:183], v[32:35], v[112:115]\n\t"
        "ds_read_b32 v56, v56\n\t"
        "v_mfma_f32_16x16x32_bf16 v[120:123], a[188:191], v[32:35], v[120:123]\n\t"
        "buffer_load_dwordx2 v[230:231], v17, s[16:19], 0 offen offset:128\n\t"
        "v_mfma_f32_16x16x32_bf16 v[120:123], a[184:187], v[20:23], v[120:123]\n\t"
        "ds_read_b128 v[32:35], v4 offset:44032\n\t"
        "s_waitcnt vmcnt(11) lgkmcnt(6)\n\t"
        "v_mfma_f32_16x16x32_bf16 v[124:127], a[184:187], v[28:31], v[124:127]\n\t"
        "ds_read_b128 v[20:23], v4 offset:45056\n\t"
        "v_lshl_or_b32 v72, v74, 4, v72\n\t"
        "v_mfma_f32_16x16x32_bf16 v[124:127], a[188:191], v[40:43], v[124:127]\n\t"
        "ds_read_b32 v60, v60\n\t"
        "v_mad_u32_u16 v50, v72, s48, v1\n\t"
        "v_mfma_f32_16x16x32_bf16 v[116:119], a[180:183], v[40:43], v[116:119]\n\t"
        "buffer_load_dword v232, v18, s[16:19], 0 offen\n\t"
        "v_mfma_f32_16x16x32_bf16 v[116:119], a[176:179], v[28:31], v[116:119]\n\t"
        "ds_read_b32 v49, v49\n\t"
        "v_add_u32 v10, s63, v9\n\t"
        "v_mfma_f32_16x16x32_bf16 v[108:111], a[168:171], v[28:31], v[108:111]\n\t"
        "ds_read_b32 v53, v53\n\t"
        "v_mad_u32_u16 v54, v72, s48, v1 op_sel:[1,0,0,0]\n\t"
        "v_mfma_f32_16x16x32_bf16 v[108:111], a[172:175], v[40:43], v[108:111]\n\t"
        "ds_read_b32 v57, v57\n\t"
        "v_lshl_or_b32 v73, v75, 4, v73\n\t"
        "v_mfma_f32_16x16x32_bf16 v[100:103], a[164:167], v[40:43], v[100:103]\n\t"
        "buffer_load_format_d16_xyzw v[80:81], v10, s[8:11], 0 offen\n\t"
        "v_mfma_f32_16x16x32_bf16 v[100:103], a[160:163], v[28:31], v[100:103]\n\t"
        "ds_read_b128 v[40:43], v4 offset:49152\n\t"
        "s_waitcnt vmcnt(11) lgkmcnt(6)\n\t"
        "v_mfma_f32_16x16x32_bf16 v[128:131], a[160:163], v[36:39], v[128:131]\n\t"
        "ds_read_b128 v[28:31], v4 offset:46080\n\t"
        "v_mad_u32_u16 v58, v73, s48, v1\n\t"
        "v_mfma_f32_16x16x32_bf16 v[128:131], a[164:167], v[24:27], v[128:131]\n\t"
        "ds_read_b32 v61, v61\n\t"
        "v_mad_u32_u16 v62, v73, s48, v1 op_sel:[1,0,0,0]\n\t"
        "v_mfma_f32_16x16x32_bf16 v[136:139], a[172:175], v[24:27], v[136:139]\n\t"
        "buffer_load_format_d16_xyzw v[82:83], v10, s[8:11], s33 offen\n\t"
        "v_mfma_f32_16x16x32_bf16 v[136:139], a[168:171], v[36:39], v[136:139]\n\t"
        "ds_read_b32 v50, v50\n\t"
        "v_lshl_or_b32 v76, v78, 4, v76\n\t"
        "v_mfma_f32_16x16x32_bf16 v[144:147], a[176:179], v[36:39], v[144:147]\n\t"
        "ds_read_b32 v54, v54\n\t"
        "v_mad_u32_u16 v51, v76, s48, v1\n\t"
        "v_mfma_f32_16x16x32_bf16 v[144:147], a[180:183], v[24:27], v[144:147]\n\t"
        "ds_read_b32 v58, v58\n\t"
        "v_mad_u32_u16 v55, v76, s48, v1 op_sel:[1,0,0,0]\n\t"
        "v_mfma_f32_16x16x32_bf16 v[152:155], a[188:191], v[24:27], v[152:155]\n\t"
        "buffer_load_format_d16_xyzw v[84:85], v10, s[8:11], s34 offen\n\t"
        "v_mfma_f32_16x16x32_bf16 v[152:155], a[184:187], v[36:39], v[152:155]\n\t"
        "ds_read_b128 v[24:27], v4 offset:50176\n\t"
        "s_waitcnt vmcnt(11) lgkmcnt(6)\n\t"
        "v_mfma_f32_16x16x32_bf16 v[156:159], a[184:187], v[44:47], v[156:159]\n\t"
        "buffer_load_format_d16_xyzw v[86:87], v10, s[8:11], s35 offen\n\t"
        "v_mfma_f32_16x16x32_bf16 v[156:159], a[188:191], v[32:35], v[156:159]\n\t"
        "ds_read_b128 v[36:39], v4 offset:47104\n\t"
        "v_lshl_or_b32 v77, v79, 4, v77\n\t"
        "v_mfma_f32_16x16x32_bf16 v[148:151], a[180:183], v[32:35], v[148:151]\n\t"
        "ds_read_b32 v62, v62\n\t"
        "v_mad_u32_u16 v59, v77, s48, v1\n\t"
        "v_mfma_f32_16x16x32_bf16 v[148:151], a[176:179], v[44:47], v[148:151]\n\t"
        "ds_read_b32 v51, v51\n\t"
        "v_mad_u32_u16 v63, v77, s48, v1 op_sel:[1,0,0,0]\n\t"
        "v_mfma_f32_16x16x32_bf16 v[140:143], a[168:171], v[44:47], v[140:143]\n\t"
        "buffer_load_format_d16_xyzw v[88:89], v10, s[8:11], s36 offen\n\t"
        "v_mfma_f32_16x16x32_bf16 v[140:143], a[172:175], v[32:35], v[140:143]\n\t"
        "ds_read_b32 v55, v55\n\t"
        "v_mfma_f32_16x16x32_bf16 v[132:135], a[164:167], v[32:35], v[132:135]\n\t"
        "ds_read_b128 v[32:35], v4 offset:51200\n\t"
        "v_mfma_f32_16x16x32_bf16 v[132:135], a[160:163], v[44:47], v[132:135]\n\t"
        "ds_read_b128 v[44:47], v4 offset:48128\n\t"
        "v_mfma_f32_16x16x32_bf16 v[160:163], a[160:163], v[20:23], v[160:163]\n\t"
        "buffer_load_format_d16_xyzw v[90:91], v10, s[8:11], s37 offen\n\t"
        "v_mfma_f32_16x16x32_bf16 v[160:163], a[164:167], v[40:43], v[160:163]\n\t"
        "ds_read_b32 v59, v59\n\t"
        "v_mfma_f32_16x16x32_bf16 v[168:171], a[172:175], v[40:43], v[168:171]\n\t"
        "ds_read_b32 v63, v63\n\t"
        "s_waitcnt vmcnt(11) lgkmcnt(5)\n\t"
        "v_mfma_f32_16x16x32_bf16 v[168:171], a[168:171], v[20:23], v[168:171]\n\t"
        "ds_write_b128 v2, v[48:51]\n\t"
        "v_mfma_f32_16x16x32_bf16 v[176:179], a[176:179], v[20:23], v[176:179]\n\t"
        "buffer_load_format_d16_xyzw v[92:93], v10, s[8:11], s38 offen\n\t"
        "v_mfma_f32_16x16x32_bf16 v[176:179], a[180:183], v[40:43], v[176:179]\n\t"
        "s_waitcnt vmcnt(11) lgkmcnt(4)\n\t"
        "v_mfma_f32_16x16x32_bf16 v[184:187], a[188:191], v[40:43], v[184:187]\n\t"
        "ds_read_b128 v[40:43], v4 offset:52224\n\t"
        "v_mfma_f32_16x16x32_bf16 v[184:187], a[184:187], v[20:23], v[184:187]\n\t"
        "buffer_load_format_d16_xyzw v[94:95], v10, s[8:11], s39 offen\n\t"
        "v_mfma_f32_16x16x32_bf16 v[188:191], a[184:187], v[28:31], v[188:191]\n\t"
        "ds_write_b128 v2, v[52:55] offset:1024\n\t"
        "v_mfma_f32_16x16x32_bf16 v[188:191], a[188:191], v[24:27], v[188:191]\n\t"
        "s_bfe_u32 s55, s72, 0x4001c\n\t"
        "v_lshl_add_u32 v247, s55, 7, v248\n\t"
        "v_mfma_f32_16x16x32_bf16 v[180:183], a[180:183], v[24:27], v[180:183]\n\t"
        "s_add_u32 m0, s53, 8192\n\t"
        "s_waitcnt vmcnt(11) lgkmcnt(3)\n\t"
        "v_mfma_f32_16x16x32_bf16 v[180:183], a[176:179], v[28:31], v[180:183]\n\t"
        "buffer_load_dwordx4 v247, s[4:7], s40 offen lds\n\t"
        "v_mfma_f32_16x16x32_bf16 v[172:175], a[168:171], v[28:31], v[172:175]\n\t"
        "buffer_load_dwordx4 v247, s[4:7], s41 offen offset:1024 lds\n\t"
        "v_mfma_f32_16x16x32_bf16 v[172:175], a[172:175], v[24:27], v[172:175]\n\t"
        "buffer_load_dwordx4 v247, s[4:7], s42 offen offset:2048 lds\n\t"
        "v_mfma_f32_16x16x32_bf16 v[164:167], a[164:167], v[24:27], v[164:167]\n\t"
        "ds_write_b128 v2, v[56:59] offset:2048\n\t"
        "v_mfma_f32_16x16x32_bf16 v[164:167], a[160:163], v[28:31], v[164:167]\n\t"
        "buffer_load_dwordx4 v247, s[4:7], s43 offen offset:3072 lds\n\t"
        "v_mfma_f32_16x16x32_bf16 v[192:195], a[160:163], v[36:39], v[192:195]\n\t"
        "s_add_u32 m0, s53, 12288\n\t"
        "v_mfma_f32_16x16x32_bf16 v[192:195], a[164:167], v[32:35], v[192:195]\n\t"
        "ds_write_b128 v2, v[60:63] offset:3072\n\t"
        "v_mfma_f32_16x16x32_bf16 v[200:203], a[172:175], v[32:35], v[200:203]\n\t"
        "buffer_load_dwordx4 v247, s[4:7], s44 offen lds\n\t"
        "v_mfma_f32_16x16x32_bf16 v[200:203], a[168:171], v[36:39], v[200:203]\n\t"
        "v_mfma_f32_16x16x32_bf16 v[208:211], a[176:179], v[36:39], v[208:211]\n\t"
        "ds_read_b128 a[192:195], v7\n\t"
        "v_mfma_f32_16x16x32_bf16 v[208:211], a[180:183], v[32:35], v[208:211]\n\t"
        "buffer_load_dwordx4 v247, s[4:7], s45 offen offset:1024 lds\n\t"
        "ds_read_b128 a[196:199], v8\n\t"
        "v_mfma_f32_16x16x32_bf16 v[216:219], a[188:191], v[32:35], v[216:219]\n\t"
        "ds_read_b128 a[200:203], v7 offset:2048\n\t"
        "v_mfma_f32_16x16x32_bf16 v[216:219], a[184:187], v[36:39], v[216:219]\n\t"
        "ds_read_b128 a[204:207], v8 offset:2048\n\t"
        "s_waitcnt vmcnt(11) lgkmcnt(5)\n\t"
        "v_mfma_f32_16x16x32_bf16 v[220:223], a[184:187], v[44:47], v[220:223]\n\t"
        "ds_read_b128 a[208:211], v7 offset:4096\n\t"
        "v_mfma_f32_16x16x32_bf16 v[220:223], a[188:191], v[40:43], v[220:223]\n\t"
        "buffer_load_dwordx4 v247, s[4:7], s46 offen offset:2048 lds\n\t"
        "ds_read_b128 a[212:215], v8 offset:4096\n\t"
        "v_mfma_f32_16x16x32_bf16 v[212:215], a[180:183], v[40:43], v[212:215]\n\t"
        "ds_read_b128 a[216:219], v7 offset:6144\n\t"
        "s_waitcnt vmcnt(11) lgkmcnt(7)\n\t"
        "ds_read_b128 a[220:223], v8 offset:6144\n\t"
        "v_mfma_f32_16x16x32_bf16 v[212:215], a[176:179], v[44:47], v[212:215]\n\t"
        "ds_read_b32 v237, v237\n\t"
        "s_barrier\n\t"
        "v_mfma_f32_16x16x32_bf16 v[204:207], a[168:171], v[44:47], v[204:207]\n\t"
        "ds_read_b128 v[20:23], v4 offset:4096\n\t"
        "v_lshl_or_b32 v80, v82, 4, v80\n\t"
        "v_mfma_f32_16x16x32_bf16 v[204:207], a[172:175], v[40:43], v[204:207]\n\t"
        "buffer_load_dwordx4 v247, s[4:7], s47 offen offset:3072 lds\n\t"
        "v_mfma_f32_16x16x32_bf16 v[196:199], a[164:167], v[40:43], v[196:199]\n\t"
        "ds_read_b128 v[24:27], v4 offset:8192\n\t"
        "v_add_u32 v10, s64, v9\n\t"
        "v_mfma_f32_16x16x32_bf16 v[196:199], a[160:163], v[44:47], v[196:199]\n\t"
        "ds_read_b128 v[28:31], v4 offset:5120\n\t"
        "v_mad_u32_u16 v48, v80, s48, v1\n\t"
        "v_mad_u32_u16 v52, v80, s48, v1 op_sel:[1,0,0,0]\n\t"
        "v_lshl_or_b32 v81, v83, 4, v81\n\t"
        "v_mad_u32_u16 v56, v81, s48, v1\n\t"
        "v_mad_u32_u16 v60, v81, s48, v1 op_sel:[1,0,0,0]\n\t"
        "v_lshl_or_b32 v84, v86, 4, v84\n\t"
        "v_mad_u32_u16 v49, v84, s48, v1\n\t"
        "v_mad_u32_u16 v53, v84, s48, v1 op_sel:[1,0,0,0]\n\t"
        "v_lshl_or_b32 v85, v87, 4, v85\n\t"
        "v_mad_u32_u16 v57, v85, s48, v1\n\t"
        "v_mad_u32_u16 v61, v85, s48, v1 op_sel:[1,0,0,0]\n\t"
        "buffer_load_format_d16_xyzw v[64:65], v10, s[8:11], 0 offen\n\t"
        "ds_read_b128 v[32:35], v4 offset:9216\n\t"
        "s_waitcnt vmcnt(11) lgkmcnt(4)\n\t"
        "ds_read_b128 v[36:39], v4 offset:6144\n\t"
        "ds_read_b128 v[40:43], v4 offset:10240\n\t"
        "buffer_load_format_d16_xyzw v[66:67], v10, s[8:11], s33 offen\n\t"
        "ds_read_b128 v[44:47], v4 offset:7168\n\t"
        "s_waitcnt vmcnt(11) lgkmcnt(5)\n\t"
        "v_mfma_f32_16x16x32_bf16 v[96:99], a[192:195], v[20:23], v[96:99]\n\t"
        "ds_read_b32 v48, v48\n\t"
        "v_mfma_f32_16x16x32_bf16 v[96:99], a[196:199], v[24:27], v[96:99]\n\t"
        "ds_read_b32 v52, v52\n\t"
        "v_mfma_f32_16x16x32_bf16 v[104:107], a[204:207], v[24:27], v[104:107]\n\t"
        "ds_read_b32 v56, v56\n\t"
        "v_mfma_f32_16x16x32_bf16 v[104:107], a[200:203], v[20:23], v[104:107]\n\t"
        "buffer_load_format_d16_xyzw v[68:69], v10, s[8:11], s34 offen\n\t"
        "v_mfma_f32_16x16x32_bf16 v[112:115], a[208:211], v[20:23], v[112:115]\n\t"
        "ds_read_b32 v60, v60\n\t"
        "s_waitcnt vmcnt(11) lgkmcnt(7)\n\t"
        "v_mfma_f32_16x16x32_bf16 v[112:115], a[212:215], v[24:27], v[112:115]\n\t"
        "ds_read_b32 v49, v49\n\t"
        "v_lshl_or_b32 v88, v90, 4, v88\n\t"
        "v_mfma_f32_16x16x32_bf16 v[120:123], a[220:223], v[24:27], v[120:123]\n\t"
        "ds_read_b128 v[24:27], v4 offset:11264\n\t"
        "v_mad_u32_u16 v50, v88, s48, v1\n\t"
        "v_mfma_f32_16x16x32_bf16 v[120:123], a[216:219], v[20:23], v[120:123]\n\t"
        "buffer_load_format_d16_xyzw v[70:71], v10, s[8:11], s35 offen\n\t"
        "v_mfma_f32_16x16x32_bf16 v[124:127], a[216:219], v[28:31], v[124:127]\n\t"
        "ds_read_b128 v[20:23], v4 offset:12288\n\t"
        "s_bfe_u32 s55, s73, 0x40000\n\t"
        "v_mfma_f32_16x16x32_bf16 v[124:127], a[220:223], v[32:35], v[124:127]\n\t"
        "ds_read_b32 v53, v53\n\t"
        "v_lshl_add_u32 v247, s55, 7, v248\n\t"
        "v_mfma_f32_16x16x32_bf16 v[116:119], a[212:215], v[32:35], v[116:119]\n\t"
        "ds_read_b32 v57, v57\n\t"
        "s_add_u32 m0, s53, 0\n\t"
        "v_mfma_f32_16x16x32_bf16 v[116:119], a[208:211], v[28:31], v[116:119]\n\t"
        "buffer_load_dwordx4 v247, s[4:7], s40 offen lds\n\t"
        "v_mfma_f32_16x16x32_bf16 v[108:111], a[200:203], v[28:31], v[108:111]\n\t"
        "ds_read_b32 v61, v61\n\t"
        "v_mad_u32_u16 v54, v88, s48, v1 op_sel:[1,0,0,0]\n\t"
        "v_mfma_f32_16x16x32_bf16 v[108:111], a[204:207], v[32:35], v[108:111]\n\t"
        "ds_read_b32 v50, v50\n\t"
        "v_lshl_or_b32 v89, v91, 4, v89\n\t"
        "v_mfma_f32_16x16x32_bf16 v[100:103], a[196:199], v[32:35], v[100:103]\n\t"
        "ds_read_b128 v[32:35], v4 offset:16384\n\t"
        "v_mad_u32_u16 v58, v89, s48, v1\n\t"
        "v_mfma_f32_16x16x32_bf16 v[100:103], a[192:195], v[28:31], v[100:103]\n\t"
        "buffer_load_dwordx4 v247, s[4:7], s41 offen offset:1024 lds\n\t"
        "s_waitcnt vmcnt(11) lgkmcnt(5)\n\t"
        "v_mfma_f32_16x16x32_bf16 v[128:131], a[192:195], v[36:39], v[128:131]\n\t"
        "ds_read_b128 v[28:31], v4 offset:13312\n\t"
        "v_mad_u32_u16 v62, v89, s48, v1 op_sel:[1,0,0,0]\n\t"
        "v_mfma_f32_16x16x32_bf16 v[128:131], a[196:199], v[40:43], v[128:131]\n\t"
        "ds_read_b32 v54, v54\n\t"
        "v_lshl_or_b32 v92, v94, 4, v92\n\t"
        "v_mfma_f32_16x16x32_bf16 v[136:139], a[204:207], v[40:43], v[136:139]\n\t"
        "ds_read_b32 v58, v58\n\t"
        "v_mad_u32_u16 v51, v92, s48, v1\n\t"
        "v_mfma_f32_16x16x32_bf16 v[136:139], a[200:203], v[36:39], v[136:139]\n\t"
        "buffer_load_dwordx4 v247, s[4:7], s42 offen offset:2048 lds\n\t"
        "v_mfma_f32_16x16x32_bf16 v[144:147], a[208:211], v[36:39], v[144:147]\n\t"
        "ds_read_b32 v62, v62\n\t"
        "s_waitcnt vmcnt(11) lgkmcnt(5)\n\t"
        "v_mfma_f32_16x16x32_bf16 v[144:147], a[212:215], v[40:43], v[144:147]\n\t"
        "ds_read_b32 v51, v51\n\t"
        "v_mad_u32_u16 v55, v92, s48, v1 op_sel:[1,0,0,0]\n\t"
        "v_mfma_f32_16x16x32_bf16 v[152:155], a[220:223], v[40:43], v[152:155]\n\t"
        "ds_read_b128 v[40:43], v4 offset:17408\n\t"
        "v_lshl_or_b32 v93, v95, 4, v93\n\t"
        "v_mfma_f32_16x16x32_bf16 v[152:155], a[216:219], v[36:39], v[152:155]\n\t"
        "buffer_load_dwordx4 v247, s[4:7], s43 offen offset:3072 lds\n\t"
        "v_mfma_f32_16x16x32_bf16 v[156:159], a[216:219], v[44:47], v[156:159]\n\t"
        "ds_read_b128 v[36:39], v4 offset:14336\n\t"
        "v_mad_u32_u16 v59, v93, s48, v1\n\t"
        "v_mfma_f32_16x16x32_bf16 v[156:159], a[220:223], v[24:27], v[156:159]\n\t"
        "ds_read_b32 v55, v55\n\t"
        "v_mad_u32_u16 v63, v93, s48, v1 op_sel:[1,0,0,0]\n\t"
        "v_mfma_f32_16x16x32_bf16 v[148:151], a[212:215], v[24:27], v[148:151]\n\t"
        "ds_read_b32 v59, v59\n\t"
        "v_mfma_f32_16x16x32_bf16 v[148:151], a[208:211], v[44:47], v[148:151]\n\t"
        "ds_read_b32 v63, v63\n\t"
        "v_mfma_f32_16x16x32_bf16 v[140:143], a[200:203], v[44:47], v[140:143]\n\t"
        "buffer_load_format_d16_xyzw v[72:73], v10, s[8:11], s36 offen\n\t"
        "v_mfma_f32_16x16x32_bf16 v[140:143], a[204:207], v[24:27], v[140:143]\n\t"
        "ds_read_b128 a[224:227], v7 offset:8192\n\t"
        "s_waitcnt vmcnt(11) lgkmcnt(6)\n\t"
        "v_mfma_f32_16x16x32_bf16 v[132:135], a[196:199], v[24:27], v[132:135]\n\t"
        "ds_read_b128 v[24:27], v4 offset:18432\n\t"
        "v_mfma_f32_16x16x32_bf16 v[132:135], a[192:195], v[44:47], v[132:135]\n\t"
        "ds_read_b128 v[44:47], v4 offset:15360\n\t"
        "v_mfma_f32_16x16x32_bf16 v[160:163], a[192:195], v[20:23], v[160:163]\n\t"
        "buffer_load_format_d16_xyzw v[74:75], v10, s[8:11], s37 offen\n\t"
        "v_mfma_f32_16x16x32_bf16 v[160:163], a[196:199], v[32:35], v[160:163]\n\t"
        "ds_read_b128 a[228:231], v8 offset:8192\n\t"
        "v_mfma_f32_16x16x32_bf16 v[168:171], a[204:207], v[32:35], v[168:171]\n\t"
        "ds_read_b128 a[232:235], v7 offset:10240\n\t"
        "v_mfma_f32_16x16x32_bf16 v[168:171], a[200:203], v[20:23], v[168:171]\n\t"
        "ds_read_b128 a[236:239], v8 offset:10240\n\t"
        "v_mfma_f32_16x16x32_bf16 v[176:179], a[208:211], v[20:23], v[176:179]\n\t"
        "buffer_load_format_d16_xyzw v[76:77], v10, s[8:11], s38 offen\n\t"
        "v_mfma_f32_16x16x32_bf16 v[176:179], a[212:215], v[32:35], v[176:179]\n\t"
        "ds_write_b32 v16, v234\n\t"
        "v_mfma_f32_16x16x32_bf16 v[184:187], a[220:223], v[32:35], v[184:187]\n\t"
        "ds_read_b128 v[32:35], v4 offset:19456\n\t"
        "s_waitcnt vmcnt(11) lgkmcnt(6)\n\t"
        "v_mfma_f32_16x16x32_bf16 v[184:187], a[216:219], v[20:23], v[184:187]\n\t"
        "ds_read_b128 a[240:243], v7 offset:12288\n\t"
        "v_mfma_f32_16x16x32_bf16 v[188:191], a[216:219], v[28:31], v[188:191]\n\t"
        "buffer_load_format_d16_xyzw v[78:79], v10, s[8:11], s39 offen\n\t"
        "v_mfma_f32_16x16x32_bf16 v[188:191], a[220:223], v[40:43], v[188:191]\n\t"
        "ds_read_b128 a[244:247], v8 offset:12288\n\t"
        "s_add_u32 m0, s53, 4096\n\t"
        "v_mfma_f32_16x16x32_bf16 v[180:183], a[212:215], v[40:43], v[180:183]\n\t"
        "ds_read_b128 a[248:251], v7 offset:14336\n\t"
        "v_mfma_f32_16x16x32_bf16 v[180:183], a[208:211], v[28:31], v[180:183]\n\t"
        "ds_read_b128 a[252:255], v8 offset:14336\n\t"
        "v_mfma_f32_16x16x32_bf16 v[172:175], a[200:203], v[28:31], v[172:175]\n\t"
        "buffer_load_dwordx4 v247, s[4:7], s44 offen lds\n\t"
        "v_mfma_f32_16x16x32_bf16 v[172:175], a[204:207], v[40:43], v[172:175]\n\t"
        "ds_write_b32 v16, v235 offset:1024\n\t"
        "v_mfma_f32_16x16x32_bf16 v[164:167], a[196:199], v[40:43], v[164:167]\n\t"
        "ds_write_b32 v16, v236 offset:16\n\t"
        "v_mfma_f32_16x16x32_bf16 v[164:167], a[192:195], v[28:31], v[164:167]\n\t"
        "ds_write_b32 v16, v237 offset:1040\n\t"
        "v_mfma_f32_16x16x32_bf16 v[192:195], a[192:195], v[36:39], v[192:195]\n\t"
        "buffer_load_dwordx4 v247, s[4:7], s45 offen offset:1024 lds\n\t"
        "v_mfma_f32_16x16x32_bf16 v[192:195], a[196:199], v[24:27], v[192:195]\n\t"
        "v_lshlrev_b32 v12, 16, v228\n\t"
        "v_and_b32 v13, 0xffff0000, v228\n\t"
        "v_mfma_f32_16x16x32_bf16 v[200:203], a[204:207], v[24:27], v[200:203]\n\t"
        "v_lshlrev_b32 v14, 16, v229\n\t"
        "v_and_b32 v15, 0xffff0000, v229\n\t"
        "v_mfma_f32_16x16x32_bf16 v[200:203], a[200:203], v[36:39], v[200:203]\n\t"
        "v_lshlrev_b32 v16, 16, v230\n\t"
        "v_and_b32 v17, 0xffff0000, v230\n\t"
        "v_mfma_f32_16x16x32_bf16 v[208:211], a[208:211], v[36:39], v[208:211]\n\t"
        "buffer_load_dwordx4 v247, s[4:7], s46 offen offset:2048 lds\n\t"
        "v_mfma_f32_16x16x32_bf16 v[208:211], a[212:215], v[24:27], v[208:211]\n\t"
        "ds_write_b128 v2, v[48:51] offset:16384\n\t"
        "v_mfma_f32_16x16x32_bf16 v[216:219], a[220:223], v[24:27], v[216:219]\n\t"
        "s_waitcnt vmcnt(11) lgkmcnt(1)\n\t"
        "v_lshl_or_b32 v64, v66, 4, v64\n\t"
        "v_mad_u32_u16 v48, v64, s48, v1\n\t"
        "v_mfma_f32_16x16x32_bf16 v[216:219], a[216:219], v[36:39], v[216:219]\n\t"
        "buffer_load_dwordx4 v247, s[4:7], s47 offen offset:3072 lds\n\t"
        "ds_read_b32 v48, v48\n\t"
        "v_mfma_f32_16x16x32_bf16 v[220:223], a[216:219], v[44:47], v[220:223]\n\t"
        "ds_write_b128 v2, v[52:55] offset:17408\n\t"
        "v_mfma_f32_16x16x32_bf16 v[220:223], a[220:223], v[32:35], v[220:223]\n\t"
        "v_add_u32 v10, s65, v9\n\t"
        "v_mad_u32_u16 v52, v64, s48, v1 op_sel:[1,0,0,0]\n\t"
        "v_lshl_or_b32 v65, v67, 4, v65\n\t"
        "v_mfma_f32_16x16x32_bf16 v[212:215], a[212:215], v[32:35], v[212:215]\n\t"
        "buffer_load_format_d16_xyzw v[80:81], v10, s[8:11], 0 offen\n\t"
        "ds_read_b32 v52, v52\n\t"
        "v_mfma_f32_16x16x32_bf16 v[212:215], a[208:211], v[44:47], v[212:215]\n\t"
        "ds_write_b128 v2, v[56:59] offset:18432\n\t"
        "v_mfma_f32_16x16x32_bf16 v[204:207], a[200:203], v[44:47], v[204:207]\n\t"
        "v_mad_u32_u16 v56, v65, s48, v1\n\t"
        "v_lshl_or_b32 v68, v70, 4, v68\n\t"
        "v_lshl_or_b32 v69, v71, 4, v69\n\t"
        "v_mfma_f32_16x16x32_bf16 v[204:207], a[204:207], v[32:35], v[204:207]\n\t"
        "buffer_load_format_d16_xyzw v[82:83], v10, s[8:11], s33 offen\n\t"
        "ds_read_b32 v56, v56\n\t"
        "v_mfma_f32_16x16x32_bf16 v[196:199], a[196:199], v[32:35], v[196:199]\n\t"
        "v_lshlrev_b32 v18, 16, v231\n\t"
        "v_and_b32 v19, 0xffff0000, v231\n\t"
        "v_lshlrev_b32 v11, 16, v232\n\t"
        "v_mfma_f32_16x16x32_bf16 v[196:199], a[192:195], v[44:47], v[196:199]\n\t"
        "v_and_b32 v0, 0xffff0000, v232\n\t"
        "buffer_load_format_d16_xyzw v[84:85], v10, s[8:11], s34 offen\n\t"
        "s_waitcnt vmcnt(11) lgkmcnt(2)\n\t"
        "ds_read_b128 v[224:227], v7\n\t"
        "ds_read_b128 v[228:231], v8\n\t"
        "ds_read_b128 v[232:235], v7 offset:2048\n\t"
        "ds_read_b128 v[236:239], v8 offset:2048\n\t"
        "buffer_load_format_d16_xyzw v[86:87], v10, s[8:11], s35 offen\n\t"
        "buffer_load_format_d16_xyzw v[88:89], v10, s[8:11], s36 offen\n\t"
        "buffer_load_format_d16_xyzw v[90:91], v10, s[8:11], s37 offen\n\t"
        "buffer_load_format_d16_xyzw v[92:93], v10, s[8:11], s38 offen\n\t"
        "ds_write_b128 v2, v[60:63] offset:19456\n\t"
        "v_mad_u32_u16 v60, v65, s48, v1 op_sel:[1,0,0,0]\n\t"
        "v_mad_u32_u16 v49, v68, s48, v1\n\t"
        "v_mad_u32_u16 v53, v68, s48, v1 op_sel:[1,0,0,0]\n\t"
        "v_mad_u32_u16 v57, v69, s48, v1\n\t"
        "v_mad_u32_u16 v61, v69, s48, v1 op_sel:[1,0,0,0]\n\t"
        "s_waitcnt vmcnt(11) lgkmcnt(1)\n\t"
        "v_lshl_or_b32 v72, v74, 4, v72\n\t"
        "v_mad_u32_u16 v50, v72, s48, v1\n\t"
        "v_mad_u32_u16 v54, v72, s48, v1 op_sel:[1,0,0,0]\n\t"
        "v_lshl_or_b32 v73, v75, 4, v73\n\t"
        "v_mad_u32_u16 v58, v73, s48, v1\n\t"
        "v_mad_u32_u16 v62, v73, s48, v1 op_sel:[1,0,0,0]\n\t"
        "v_lshl_or_b32 v76, v78, 4, v76\n\t"
        "v_mad_u32_u16 v51, v76, s48, v1\n\t"
        "v_mad_u32_u16 v55, v76, s48, v1 op_sel:[1,0,0,0]\n\t"
        "v_lshl_or_b32 v77, v79, 4, v77\n\t"
        "buffer_load_format_d16_xyzw v[94:95], v10, s[8:11], s39 offen\n\t"
        "ds_read_b32 v60, v60\n\t"
        "v_add_u32 v10, s66, v9\n\t"
        "v_mad_u32_u16 v59, v77, s48, v1\n\t"
        "v_mad_u32_u16 v63, v77, s48, v1 op_sel:[1,0,0,0]\n\t"
        "buffer_load_format_d16_xyzw v[64:65], v10, s[8:11], 0 offen\n\t"
        "ds_read_b32 v49, v49\n\t"
        "ds_read_b32 v53, v53\n\t"
        "ds_read_b32 v57, v57\n\t"
        "s_waitcnt vmcnt(11) lgkmcnt(4)\n\t"
        "ds_read_b32 v61, v61\n\t"
        "s_barrier\n\t"
        "ds_read_b128 v[20:23], v4 offset:20480\n\t"
        "ds_read_b128 v[40:43], v4 offset:24576\n\t"
        "ds_read_b128 v[28:31], v4 offset:21504\n\t"
        "buffer_load_format_d16_xyzw v[66:67], v10, s[8:11], s33 offen\n\t"
        "ds_read_b128 v[24:27], v4 offset:25600\n\t"
        "ds_read_b128 v[36:39], v4 offset:22528\n\t"
        "ds_read_b128 v[32:35], v4 offset:26624\n\t"
        "ds_read_b128 v[44:47], v4 offset:23552\n\t"
        "buffer_load_format_d16_xyzw v[68:69], v10, s[8:11], s34 offen\n\t"
        "ds_read_b32 v50, v50\n\t"
        "s_waitcnt vmcnt(11) lgkmcnt(8)\n\t"
        "ds_read_b32 v54, v54\n\t"
        "ds_read_b32 v58, v58\n\t"
        "ds_read_b32 v62, v62\n\t"
        "s_waitcnt lgkmcnt(9)\n\t"
        "v_mfma_f32_16x16x32_bf16 v[96:99], a[224:227], v[20:23], v[96:99]\n\t"
        "ds_read_b32 v51, v51\n\t"
        "v_mfma_f32_16x16x32_bf16 v[96:99], a[228:231], v[40:43], v[96:99]\n\t"
        "buffer_load_format_d16_xyzw v[70:71], v10, s[8:11], s35 offen\n\t"
        "v_mfma_f32_16x16x32_bf16 v[104:107], a[236:239], v[40:43], v[104:107]\n\t"
        "ds_read_b128 v[40:43], v4 offset:27648\n\t"
        "v_mfma_f32_16x16x32_bf16 v[104:107], a[232:235], v[20:23], v[104:107]\n\t"
        "ds_read_b128 v[20:23], v4 offset:28672\n\t"
        "s_waitcnt vmcnt(11) lgkmcnt(9)\n\t"
        "v_mfma_f32_16x16x32_bf16 v[108:111], a[232:235], v[28:31], v[108:111]\n\t"
        "ds_read_b32 v55, v55\n\t"
        "v_mfma_f32_16x16x32_bf16 v[108:111], a[236:239], v[24:27], v[108:111]\n\t"
        "buffer_load_format_d16_xyzw v[72:73], v10, s[8:11], s36 offen\n\t"
        "v_mfma_f32_16x16x32_bf16 v[100:103], a[228:231], v[24:27], v[100:103]\n\t"
        "ds_read_b128 v[24:27], v4 offset:32768\n\t"
        "v_mfma_f32_16x16x32_bf16 v[100:103], a[224:227], v[28:31], v[100:103]\n\t"
        "ds_read_b128 v[28:31], v4 offset:29696\n\t"
        "s_waitcnt vmcnt(11) lgkmcnt(5)\n\t"
        "v_mfma_f32_16x16x32_bf16 v[128:131], a[224:227], v[36:39], v[128:131]\n\t"
        "ds_read_b32 v59, v59\n\t"
        "v_mfma_f32_16x16x32_bf16 v[128:131], a[228:231], v[32:35], v[128:131]\n\t"
        "ds_read_b32 v63, v63\n\t"
        "v_mfma_f32_16x16x32_bf16 v[136:139], a[236:239], v[32:35], v[136:139]\n\t"
        "buffer_load_format_d16_xyzw v[74:75], v10, s[8:11], s37 offen\n\t"
        "v_mfma_f32_16x16x32_bf16 v[136:139], a[232:235], v[36:39], v[136:139]\n\t"
        "ds_read_b128 v[32:35], v4 offset:33792\n\t"
        "s_waitcnt vmcnt(11) lgkmcnt(5)\n\t"
        "v_mfma_f32_16x16x32_bf16 v[140:143], a[232:235], v[44:47], v[140:143]\n\t"
        "ds_read_b128 v[36:39], v4 offset:30720\n\t"
        ".Lp12_%=:\n\t"
        "s_cbranch_execz .Lret0_%=\n\t"
        "v_mfma_f32_16x16x32_bf16 v[140:143], a[236:239], v[40:43], v[140:143]\n\t"
        "ds_write_b128 v2, v[48:51] offset:32768\n\t"
        "v_mfma_f32_16x16x32_bf16 v[132:135], a[228:231], v[40:43], v[132:135]\n\t"
        "buffer_load_format_d16_xyzw v[76:77], v10, s[8:11], s38 offen\n\t"
        "v_mfma_f32_16x16x32_bf16 v[132:135], a[224:227], v[44:47], v[132:135]\n\t"
        "s_waitcnt vmcnt(11) lgkmcnt(4)\n\t"
        "v_lshl_or_b32 v80, v82, 4, v80\n\t"
        "v_mfma_f32_16x16x32_bf16 v[96:99], v[224:227], v[20:23], v[96:99]\n\t"
        "ds_read_b128 v[40:43], v4 offset:34816\n\t"
        "v_mad_u32_u16 v48, v80, s48, v1\n\t"
        "v_mfma_f32_16x16x32_bf16 v[96:99], v[228:231], v[24:27], v[96:99]\n\t"
        "buffer_load_format_d16_xyzw v[78:79], v10, s[8:11], s39 offen\n\t"
        "s_waitcnt vmcnt(11) lgkmcnt(3)\n\t"
        "v_mfma_f32_16x16x32_bf16 v[100:103], v[228:231], v[32:35], v[100:103]\n\t"
        "ds_read_b128 v[44:47], v4 offset:31744\n\t"
        "v_lshl_or_b32 v81, v83, 4, v81\n\t"
        "v_mfma_f32_16x16x32_bf16 v[100:103], v[224:227], v[28:31], v[100:103]\n\t"
        "ds_read_b128 v[240:243], v7 offset:4096\n\t"
        "v_lshl_or_b32 v84, v86, 4, v84\n\t"
        "v_lshl_or_b32 v85, v87, 4, v85\n\t"
        "v_mfma_f32_16x16x32_bf16 v[104:107], v[232:235], v[20:23], v[104:107]\n\t"
        "ds_read_b128 v[20:23], v4 offset:35840\n\t"
        "v_mfma_f32_16x16x32_bf16 v[104:107], v[236:239], v[24:27], v[104:107]\n\t"
        "ds_read_b128 v[244:247], v8 offset:4096\n\t"
        "v_mul_f32 v96, v96, v12\n\t"
        "v_mul_f32 v100, v100, v13\n\t"
        "v_cvt_pk_bf16_f32 v96, v96, v100\n\t"
        "v_mfma_f32_16x16x32_bf16 v[108:111], v[236:239], v[32:35], v[108:111]\n\t"
        "ds_read_b128 v[248:251], v7 offset:6144\n\t"
        "v_mul_f32 v97, v97, v12\n\t"
        "v_mul_f32 v101, v101, v13\n\t"
        "v_cvt_pk_bf16_f32 v100, v97, v101\n\t"
        "v_mfma_f32_16x16x32_bf16 v[108:111], v[232:235], v[28:31], v[108:111]\n\t"
        "ds_read_b128 v[252:255], v8 offset:6144\n\t"
        "v_mul_f32 v98, v98, v12\n\t"
        "v_mul_f32 v102, v102, v13\n\t"
        "s_waitcnt lgkmcnt(6)\n\t"
        "v_mfma_f32_16x16x32_bf16 v[128:131], v[224:227], v[36:39], v[128:131]\n\t"
        "ds_read_b128 v[24:27], v4 offset:20480\n\t"
        "v_cvt_pk_bf16_f32 v98, v98, v102\n\t"
        "v_mul_f32 v99, v99, v12\n\t"
        "v_mul_f32 v103, v103, v13\n\t"
        "v_mfma_f32_16x16x32_bf16 v[128:131], v[228:231], v[40:43], v[128:131]\n\t"
        "ds_read_b128 v[32:35], v4 offset:24576\n\t"
        "v_cvt_pk_bf16_f32 v102, v99, v103\n\t"
        "s_waitcnt lgkmcnt(5)\n\t"
        "v_mfma_f32_16x16x32_bf16 v[132:135], v[228:231], v[20:23], v[132:135]\n\t"
        "ds_write_b128 v2, v[52:55] offset:33792\n\t"
        "v_mfma_f32_16x16x32_bf16 v[132:135], v[224:227], v[44:47], v[132:135]\n\t"
        "v_mad_u32_u16 v52, v80, s48, v1 op_sel:[1,0,0,0]\n\t"
        "s_waitcnt vmcnt(8) lgkmcnt(4)\n\t"
        "v_lshl_or_b32 v88, v90, 4, v88\n\t"
        "v_lshl_or_b32 v89, v91, 4, v89\n\t"
        "v_mfma_f32_16x16x32_bf16 v[136:139], v[232:235], v[36:39], v[136:139]\n\t"
        "ds_read_b128 v[28:31], v4 offset:21504\n\t"
        "v_lshl_or_b32 v92, v94, 4, v92\n\t"
        "v_lshl_or_b32 v93, v95, 4, v93\n\t"
        "v_mfma_f32_16x16x32_bf16 v[136:139], v[236:239], v[40:43], v[136:139]\n\t"
        "ds_read_b128 v[36:39], v4 offset:25600\n\t"
        "v_mul_f32 v128, v128, v14\n\t"
        "v_mul_f32 v132, v132, v15\n\t"
        "v_cvt_pk_bf16_f32 v97, v128, v132\n\t"
        "v_mfma_f32_16x16x32_bf16 v[140:143], v[236:239], v[20:23], v[140:143]\n\t"
        "buffer_store_dwordx2 v[96:97], v5, s[12:15], s57 offen sc1\n\t"
        "v_mfma_f32_16x16x32_bf16 v[140:143], v[232:235], v[44:47], v[140:143]\n\t"
        "ds_write_b128 v2, v[56:59] offset:34816\n\t"
        "s_waitcnt lgkmcnt(4)\n\t"
        "v_mfma_f32_16x16x32_bf16 v[112:115], a[240:243], v[24:27], v[112:115]\n\t"
        "v_mul_f32 v129, v129, v14\n\t"
        "v_mul_f32 v133, v133, v15\n\t"
        "v_cvt_pk_bf16_f32 v101, v129, v133\n\t"
        "v_mul_f32 v130, v130, v14\n\t"
        "v_mfma_f32_16x16x32_bf16 v[112:115], a[244:247], v[32:35], v[112:115]\n\t"
        "ds_read_b128 v[40:43], v4 offset:22528\n\t"
        "v_mul_f32 v134, v134, v15\n\t"
        "v_cvt_pk_bf16_f32 v99, v130, v134\n\t"
        "v_mul_f32 v131, v131, v14\n\t"
        "v_mfma_f32_16x16x32_bf16 v[120:123], a[252:255], v[32:35], v[120:123]\n\t"
        "ds_read_b128 v[20:23], v4 offset:26624\n\t"
        "buffer_store_dwordx2 v[100:101], v5, s[12:15], s58 offen sc1\n\t"
        "v_mfma_f32_16x16x32_bf16 v[120:123], a[248:251], v[24:27], v[120:123]\n\t"
        "ds_write_b128 v2, v[60:63] offset:35840\n\t"
        "s_waitcnt lgkmcnt(4)\n\t"
        "v_mfma_f32_16x16x32_bf16 v[124:127], a[248:251], v[28:31], v[124:127]\n\t"
        "v_mul_f32 v135, v135, v15\n\t"
        "v_cvt_pk_bf16_f32 v103, v131, v135\n\t"
        "v_mfma_f32_16x16x32_bf16 v[124:127], a[252:255], v[36:39], v[124:127]\n\t"
        "v_mul_f32 v104, v104, v12\n\t"
        "v_mul_f32 v108, v108, v13\n\t"
        "v_mfma_f32_16x16x32_bf16 v[116:119], a[244:247], v[36:39], v[116:119]\n\t"
        "ds_read_b128 v[44:47], v4 offset:23552\n\t"
        "v_cvt_pk_bf16_f32 v104, v104, v108\n\t"
        "v_mfma_f32_16x16x32_bf16 v[116:119], a[240:243], v[28:31], v[116:119]\n\t"
        "ds_read_b128 v[32:35], v4 offset:27648\n\t"
        "buffer_store_dwordx2 v[98:99], v5, s[12:15], s59 offen sc1\n\t"
        "s_waitcnt lgkmcnt(3)\n\t"
        "v_mfma_f32_16x16x32_bf16 v[144:147], a[240:243], v[40:43], v[144:147]\n\t"
        "ds_read_b128 v[24:27], v4 offset:28672\n\t"
        "v_mul_f32 v105, v105, v12\n\t"
        "v_mfma_f32_16x16x32_bf16 v[144:147], a[244:247], v[20:23], v[144:147]\n\t"
        "ds_read_b128 v[36:39], v4 offset:32768\n\t"
        "v_mul_f32 v109, v109, v13\n\t"
        "v_mfma_f32_16x16x32_bf16 v[152:155], a[252:255], v[20:23], v[152:155]\n\t"
        "ds_read_b128 v[28:31], v4 offset:29696\n\t"
        "v_cvt_pk_bf16_f32 v108, v105, v109\n\t"
        "v_mfma_f32_16x16x32_bf16 v[152:155], a[248:251], v[40:43], v[152:155]\n\t"
        "ds_read_b128 v[20:23], v4 offset:33792\n\t"
        "v_mul_f32 v106, v106, v12\n\t"
        "v_mul_f32 v110, v110, v13\n\t"
        "v_cvt_pk_bf16_f32 v106, v106, v110\n\t"
        "v_mul_f32 v107, v107, v12\n\t"
        "v_mul_f32 v111, v111, v13\n\t"
        "v_cvt_pk_bf16_f32 v110, v107, v111\n\t"
        "v_mul_f32 v136, v136, v14\n\t"
        "v_mul_f32 v140, v140, v15\n\t"
        "v_cvt_pk_bf16_f32 v105, v136, v140\n\t"
        "s_waitcnt lgkmcnt(4)\n\t"
        "v_mfma_f32_16x16x32_bf16 v[156:159], a[248:251], v[44:47], v[156:159]\n\t"
        "buffer_store_dwordx2 v[102:103], v5, s[12:15], s60 offen sc1\n\t"
        "v_mfma_f32_16x16x32_bf16 v[156:159], a[252:255], v[32:35], v[156:159]\n\t"
        "ds_read_b32 v48, v48\n\t"
        "v_mul_f32 v137, v137, v14\n\t"
        "v_mfma_f32_16x16x32_bf16 v[148:151], a[244:247], v[32:35], v[148:151]\n\t"
        "ds_read_b32 v52, v52\n\t"
        "v_mul_f32 v141, v141, v15\n\t"
        "v_mfma_f32_16x16x32_bf16 v[148:151], a[240:243], v[44:47], v[148:151]\n\t"
        "ds_read_b128 v[40:43], v4 offset:30720\n\t"
        "s_waitcnt lgkmcnt(4)\n\t"
        "v_mfma_f32_16x16x32_bf16 v[112:115], v[240:243], v[24:27], v[112:115]\n\t"
        "ds_read_b128 v[32:35], v4 offset:34816\n\t"
        "v_cvt_pk_bf16_f32 v109, v137, v141\n\t"
        "v_mfma_f32_16x16x32_bf16 v[112:115], v[244:247], v[36:39], v[112:115]\n\t"
        "ds_read_b128 v[44:47], v4 offset:31744\n\t"
        "s_waitcnt lgkmcnt(5)\n\t"
        "v_mfma_f32_16x16x32_bf16 v[116:119], v[244:247], v[20:23], v[116:119]\n\t"
        "v_mul_f32 v138, v138, v14\n\t"
        "v_mul_f32 v142, v142, v15\n\t"
        "v_mfma_f32_16x16x32_bf16 v[116:119], v[240:243], v[28:31], v[116:119]\n\t"
        "buffer_store_dwordx2 v[104:105], v5, s[12:15], s61 offen sc1\n\t"
        "v_mfma_f32_16x16x32_bf16 v[120:123], v[248:251], v[24:27], v[120:123]\n\t"
        "ds_read_b128 v[24:27], v4 offset:35840\n\t"
        "v_cvt_pk_bf16_f32 v107, v138, v142\n\t"
        "v_mfma_f32_16x16x32_bf16 v[120:123], v[252:255], v[36:39], v[120:123]\n\t"
        "v_mul_f32 v139, v139, v14\n\t"
        "v_mul_f32 v143, v143, v15\n\t"
        "v_mfma_f32_16x16x32_bf16 v[124:127], v[252:255], v[20:23], v[124:127]\n\t"
        "v_cvt_pk_bf16_f32 v111, v139, v143\n\t"
        "s_barrier\n\t"
        "v_mfma_f32_16x16x32_bf16 v[124:127], v[248:251], v[28:31], v[124:127]\n\t"
        "ds_read_b128 v[36:39], v4 offset:36864\n\t"
        "s_waitcnt lgkmcnt(3)\n\t"
        "v_mfma_f32_16x16x32_bf16 v[144:147], v[240:243], v[40:43], v[144:147]\n\t"
        "ds_read_b128 v[20:23], v4 offset:40960\n\t"
        "v_mad_u32_u16 v56, v81, s48, v1\n\t"
        "v_mfma_f32_16x16x32_bf16 v[144:147], v[244:247], v[32:35], v[144:147]\n\t"
        "ds_read_b32 v56, v56\n\t"
        "buffer_store_dwordx2 v[108:109], v5, s[12:15], s62 offen sc1\n\t"
        "ds_read_b128 v[28:31], v4 offset:45056\n\t"
        "v_mul_f32 v112, v112, v12\n\t"
        "v_mul_f32 v116, v116, v13\n\t"
        "v_cvt_pk_bf16_f32 v112, v112, v116\n\t"
        "v_mul_f32 v113, v113, v12\n\t"
        "s_waitcnt lgkmcnt(4)\n\t"
        "v_mfma_f32_16x16x32_bf16 v[148:151], v[244:247], v[24:27], v[148:151]\n\t"
        "v_mul_f32 v117, v117, v13\n\t"
        "v_cvt_pk_bf16_f32 v116, v113, v117\n\t"
        "v_mfma_f32_16x16x32_bf16 v[148:151], v[240:243], v[44:47], v[148:151]\n\t"
        "v_mul_f32 v114, v114, v12\n\t"
        "v_mul_f32 v118, v118, v13\n\t"
        "v_mfma_f32_16x16x32_bf16 v[152:155], v[248:251], v[40:43], v[152:155]\n\t"
        "ds_read_b128 v[40:43], v4 offset:49152\n\t"
        "v_cvt_pk_bf16_f32 v114, v114, v118\n\t"
        "v_mfma_f32_16x16x32_bf16 v[152:155], v[252:255], v[32:35], v[152:155]\n\t"
        "ds_read_b128 v[32:35], v4 offset:37888\n\t"
        "v_mul_f32 v115, v115, v12\n\t"
        "v_mfma_f32_16x16x32_bf16 v[156:159], v[252:255], v[24:27], v[156:159]\n\t"
        "buffer_store_dwordx2 v[106:107], v5, s[12:15], s68 offen sc1\n\t"
        "v_mfma_f32_16x16x32_bf16 v[156:159], v[248:251], v[44:47], v[156:159]\n\t"
        "ds_read_b128 v[24:27], v4 offset:41984\n\t"
        "s_waitcnt lgkmcnt(3)\n\t"
        "v_mfma_f32_16x16x32_bf16 v[160:163], a[224:227], v[36:39], v[160:163]\n\t"
        "ds_read_b128 v[44:47], v4 offset:46080\n\t"
        "v_mul_f32 v119, v119, v13\n\t"
        "v_mfma_f32_16x16x32_bf16 v[160:163], a[228:231], v[20:23], v[160:163]\n\t"
        "v_cvt_pk_bf16_f32 v118, v115, v119\n\t"
        "v_mul_f32 v144, v144, v14\n\t"
        "v_mul_f32 v148, v148, v15\n\t"
        "v_cvt_pk_bf16_f32 v113, v144, v148\n\t"
        "v_mul_f32 v145, v145, v14\n\t"
        "v_mul_f32 v149, v149, v15\n\t"
        "v_cvt_pk_bf16_f32 v117, v145, v149\n\t"
        "v_mad_u32_u16 v60, v81, s48, v1 op_sel:[1,0,0,0]\n\t"
        "s_waitcnt lgkmcnt(3)\n\t"
        "v_mfma_f32_16x16x32_bf16 v[160:163], v[224:227], v[28:31], v[160:163]\n\t"
        "ds_read_b32 v60, v60\n\t"
        "v_mad_u32_u16 v49, v84, s48, v1\n\t"
        "v_mfma_f32_16x16x32_bf16 v[160:163], v[228:231], v[40:43], v[160:163]\n\t"
        "buffer_store_dwordx2 v[110:111], v5, s[12:15], s69 offen sc1\n\t"
        "v_mfma_f32_16x16x32_bf16 v[168:171], v[236:239], v[40:43], v[168:171]\n\t"
        "ds_read_b32 v49, v49\n\t"
        "v_mul_f32 v146, v146, v14\n\t"
        "v_mfma_f32_16x16x32_bf16 v[168:171], v[232:235], v[28:31], v[168:171]\n\t"
        "v_mul_f32 v150, v150, v15\n\t"
        "v_cvt_pk_bf16_f32 v115, v146, v150\n\t"
        "v_mfma_f32_16x16x32_bf16 v[168:171], a[236:239], v[20:23], v[168:171]\n\t"
        "v_mad_u32_u16 v53, v84, s48, v1 op_sel:[1,0,0,0]\n\t"
        "v_mad_u32_u16 v57, v85, s48, v1\n\t"
        "v_mfma_f32_16x16x32_bf16 v[168:171], a[232:235], v[36:39], v[168:171]\n\t"
        "ds_read_b32 v53, v53\n\t"
        "v_mad_u32_u16 v61, v85, s48, v1 op_sel:[1,0,0,0]\n\t"
        "v_mfma_f32_16x16x32_bf16 v[176:179], a[240:243], v[36:39], v[176:179]\n\t"
        "ds_read_b32 v57, v57\n\t"
        "v_mad_u32_u16 v50, v88, s48, v1\n\t"
        "v_mfma_f32_16x16x32_bf16 v[176:179], a[244:247], v[20:23], v[176:179]\n\t"
        "ds_read_b32 v61, v61\n\t"
        "v_mad_u32_u16 v54, v88, s48, v1 op_sel:[1,0,0,0]\n\t"
        "v_mfma_f32_16x16x32_bf16 v[176:179], v[240:243], v[28:31], v[176:179]\n\t"
        "buffer_store_dwordx2 v[112:113], v5, s[12:15], s70 offen sc1\n\t"
        "v_mfma_f32_16x16x32_bf16 v[176:179], v[244:247], v[40:43], v[176:179]\n\t"
        "ds_read_b32 v50, v50\n\t"
        "v_mul_f32 v147, v147, v14\n\t"
        "v_mfma_f32_16x16x32_bf16 v[184:187], v[252:255], v[40:43], v[184:187]\n\t"
        "ds_read_b128 v[40:43], v4 offset:50176\n\t"
        "v_mul_f32 v151, v151, v15\n\t"
        "v_mfma_f32_16x16x32_bf16 v[184:187], v[248:251], v[28:31], v[184:187]\n\t"
        "ds_read_b32 v54, v54\n\t"
        "v_cvt_pk_bf16_f32 v119, v147, v151\n\t"
        "v_mfma_f32_16x16x32_bf16 v[184:187], a[252:255], v[20:23], v[184:187]\n\t"
        "ds_read_b128 v[28:31], v4 offset:38912\n\t"
        "v_mad_u32_u16 v58, v89, s48, v1\n\t"
        "v_mfma_f32_16x16x32_bf16 v[184:187], a[248:251], v[36:39], v[184:187]\n\t"
        "ds_read_b32 v58, v58\n\t"
        "s_waitcnt lgkmcnt(6)\n\t"
        "v_mfma_f32_16x16x32_bf16 v[188:191], a[248:251], v[32:35], v[188:191]\n\t"
        "ds_read_b128 v[20:23], v4 offset:43008\n\t"
        "v_mad_u32_u16 v62, v89, s48, v1 op_sel:[1,0,0,0]\n\t"
        "v_mfma_f32_16x16x32_bf16 v[188:191], a[252:255], v[24:27], v[188:191]\n\t"
        "buffer_store_dwordx2 v[116:117], v5, s[12:15], s71 offen sc1\n\t"
        "ds_read_b32 v62, v62\n\t"
        "v_mul_f32 v120, v120, v12\n\t"
        "v_mul_f32 v124, v124, v13\n\t"
        "v_cvt_pk_bf16_f32 v120, v120, v124\n\t"
        "v_mul_f32 v121, v121, v12\n\t"
        "v_mul_f32 v125, v125, v13\n\t"
        "s_waitcnt lgkmcnt(5)\n\t"
        "v_mfma_f32_16x16x32_bf16 v[188:191], v[248:251], v[44:47], v[188:191]\n\t"
        "ds_read_b128 v[36:39], v4 offset:47104\n\t"
        "v_cvt_pk_bf16_f32 v124, v121, v125\n\t"
        "v_mfma_f32_16x16x32_bf16 v[188:191], v[252:255], v[40:43], v[188:191]\n\t"
        "v_mul_f32 v122, v122, v12\n\t"
        "v_mul_f32 v126, v126, v13\n\t"
        "v_mfma_f32_16x16x32_bf16 v[180:183], v[244:247], v[40:43], v[180:183]\n\t"
        "v_cvt_pk_bf16_f32 v122, v122, v126\n\t"
        "v_mul_f32 v123, v123, v12\n\t"
        "v_mfma_f32_16x16x32_bf16 v[180:183], v[240:243], v[44:47], v[180:183]\n\t"
        "v_mul_f32 v127, v127, v13\n\t"
        "v_cvt_pk_bf16_f32 v126, v123, v127\n\t"
        "v_mfma_f32_16x16x32_bf16 v[180:183], a[244:247], v[24:27], v[180:183]\n\t"
        "buffer_store_dwordx2 v[114:115], v5, s[12:15], s88 offen sc1\n\t"
        "v_mfma_f32_16x16x32_bf16 v[180:183], a[240:243], v[32:35], v[180:183]\n\t"
        "v_mul_f32 v152, v152, v14\n\t"
        "v_mul_f32 v156, v156, v15\n\t"
        "v_mfma_f32_16x16x32_bf16 v[172:175], a[232:235], v[32:35], v[172:175]\n\t"
        "v_cvt_pk_bf16_f32 v121, v152, v156\n\t"
        "v_mul_f32 v153, v153, v14\n\t"
        "v_mfma_f32_16x16x32_bf16 v[172:175], a[236:239], v[24:27], v[172:175]\n\t"
        "v_mul_f32 v157, v157, v15\n\t"
        "v_cvt_pk_bf16_f32 v125, v153, v157\n\t"
        "v_mfma_f32_16x16x32_bf16 v[172:175], v[232:235], v[44:47], v[172:175]\n\t"
        "v_mad_u32_u16 v51, v92, s48, v1\n\t"
        "v_mad_u32_u16 v55, v92, s48, v1 op_sel:[1,0,0,0]\n\t"
        "v_mfma_f32_16x16x32_bf16 v[172:175], v[236:239], v[40:43], v[172:175]\n\t"
        "ds_read_b32 v51, v51\n\t"
        "v_mad_u32_u16 v59, v93, s48, v1\n\t"
        "v_mfma_f32_16x16x32_bf16 v[164:167], v[228:231], v[40:43], v[164:167]\n\t"
        "ds_read_b128 v[40:43], v4 offset:51200\n\t"
        "v_mad_u32_u16 v63, v93, s48, v1 op_sel:[1,0,0,0]\n\t"
        "v_mfma_f32_16x16x32_bf16 v[164:167], v[224:227], v[44:47], v[164:167]\n\t"
        "buffer_store_dwordx2 v[118:119], v5, s[12:15], s89 offen sc1\n\t"
        "v_mfma_f32_16x16x32_bf16 v[164:167], a[228:231], v[24:27], v[164:167]\n\t"
        "ds_read_b32 v55, v55\n\t"
        "v_mul_f32 v154, v154, v14\n\t"
        "v_mfma_f32_16x16x32_bf16 v[164:167], a[224:227], v[32:35], v[164:167]\n\t"
        "ds_read_b32 v59, v59\n\t"
        "s_waitcnt lgkmcnt(4)\n\t"
        "v_mfma_f32_16x16x32_bf16 v[192:195], a[224:227], v[28:31], v[192:195]\n\t"
        "ds_read_b32 v63, v63\n\t"
        "v_mul_f32 v158, v158, v15\n\t"
        "v_mfma_f32_16x16x32_bf16 v[192:195], a[228:231], v[20:23], v[192:195]\n\t"
        "ds_read_b128 v[80:83], v4 offset:39936\n\t"
        "v_cvt_pk_bf16_f32 v123, v154, v158\n\t"
        "s_waitcnt lgkmcnt(5)\n\t"
        "s_waitcnt vmcnt(16)\n\t"
        "ds_write_b128 v3, v[48:51]\n\t"
        "s_waitcnt lgkmcnt(5)\n\t"
        "v_mfma_f32_16x16x32_bf16 v[192:195], v[224:227], v[36:39], v[192:195]\n\t"
        "buffer_store_dwordx2 v[120:121], v5, s[12:15], s90 offen sc1\n\t"
        "v_mfma_f32_16x16x32_bf16 v[192:195], v[228:231], v[40:43], v[192:195]\n\t"
        "ds_read_b128 v[84:87], v4 offset:44032\n\t"
        "v_mul_f32 v155, v155, v14\n\t"
        "v_mfma_f32_16x16x32_bf16 v[200:203], v[236:239], v[40:43], v[200:203]\n\t"
        "ds_read_b128 v[88:91], v4 offset:48128\n\t"
        "v_mul_f32 v159, v159, v15\n\t"
        "v_mfma_f32_16x16x32_bf16 v[200:203], v[232:235], v[36:39], v[200:203]\n\t"
        "ds_read_b128 v[92:95], v4 offset:52224\n\t"
        "v_cvt_pk_bf16_f32 v127, v155, v159\n\t"
        "v_mfma_f32_16x16x32_bf16 v[200:203], a[236:239], v[20:23], v[200:203]\n\t"
        "ds_read_b128 v[44:47], v4\n\t"
        "s_waitcnt lgkmcnt(5)\n\t"
        "v_mfma_f32_16x16x32_bf16 v[200:203], a[232:235], v[28:31], v[200:203]\n\t"
        "ds_write_b128 v3, v[52:55] offset:1024\n\t"
        "v_mfma_f32_16x16x32_bf16 v[208:211], a[240:243], v[28:31], v[208:211]\n\t"
        "buffer_store_dwordx2 v[124:125], v5, s[12:15], s91 offen sc1\n\t"
        "v_mfma_f32_16x16x32_bf16 v[208:211], a[244:247], v[20:23], v[208:211]\n\t"
        "v_mul_f32 v184, v184, v16\n\t"
        "v_mul_f32 v188, v188, v17\n\t"
        "v_mfma_f32_16x16x32_bf16 v[208:211], v[240:243], v[36:39], v[208:211]\n\t"
        "ds_write_b128 v3, v[56:59] offset:16\n\t"
        "v_mfma_f32_16x16x32_bf16 v[208:211], v[244:247], v[40:43], v[208:211]\n\t"
        "v_cvt_pk_bf16_f32 v184, v184, v188\n\t"
        "v_mul_f32 v185, v185, v16\n\t"
        "v_mfma_f32_16x16x32_bf16 v[216:219], v[252:255], v[40:43], v[216:219]\n\t"
        "v_mul_f32 v189, v189, v17\n\t"
        "v_cvt_pk_bf16_f32 v188, v185, v189\n\t"
        "v_mfma_f32_16x16x32_bf16 v[216:219], v[248:251], v[36:39], v[216:219]\n\t"
        "ds_write_b128 v3, v[60:63] offset:1040\n\t"
        "v_mfma_f32_16x16x32_bf16 v[216:219], a[252:255], v[20:23], v[216:219]\n\t"
        "buffer_store_dwordx2 v[122:123], v5, s[12:15], s54 offen sc1\n\t"
        "v_mfma_f32_16x16x32_bf16 v[216:219], a[248:251], v[28:31], v[216:219]\n\t"
        "v_mul_f32 v186, v186, v16\n\t"
        "s_waitcnt lgkmcnt(2)\n\t"
        "v_mfma_f32_16x16x32_bf16 v[220:223], a[248:251], v[80:83], v[220:223]\n\t"
        "ds_read_b128 v[24:27], v4 offset:2048\n\t"
        "v_mul_f32 v190, v190, v17\n\t"
        "v_mfma_f32_16x16x32_bf16 v[220:223], a[252:255], v[84:87], v[220:223]\n\t"
        "v_cvt_pk_bf16_f32 v186, v186, v190\n\t"
        "v_mul_f32 v187, v187, v16\n\t"
        "v_mfma_f32_16x16x32_bf16 v[220:223], v[248:251], v[88:91], v[220:223]\n\t"
        "v_mul_f32 v191, v191, v17\n\t"
        "v_cvt_pk_bf16_f32 v190, v187, v191\n\t"
        "v_mfma_f32_16x16x32_bf16 v[220:223], v[252:255], v[92:95], v[220:223]\n\t"
        "v_lshl_or_b32 v64, v66, 4, v64\n\t"
        "v_mad_u32_u16 v48, v64, s48, v1\n\t"
        "v_mfma_f32_16x16x32_bf16 v[212:215], v[244:247], v[92:95], v[212:215]\n\t"
        "ds_read_b32 v48, v48\n\t"
        "v_mad_u32_u16 v52, v64, s48, v1 op_sel:[1,0,0,0]\n\t"
        "v_mfma_f32_16x16x32_bf16 v[212:215], v[240:243], v[88:91], v[212:215]\n\t"
        "buffer_store_dwordx2 v[126:127], v5, s[12:15], s3 offen sc1\n\t"
        "v_mfma_f32_16x16x32_bf16 v[212:215], a[244:247], v[84:87], v[212:215]\n\t"
        "ds_read_b32 v52, v52\n\t"
        "v_mul_f32 v216, v216, v18\n\t"
        "v_mfma_f32_16x16x32_bf16 v[212:215], a[240:243], v[80:83], v[212:215]\n\t"
        "v_mul_f32 v220, v220, v19\n\t"
        "v_cvt_pk_bf16_f32 v185, v216, v220\n\t"
        "v_mfma_f32_16x16x32_bf16 v[204:207], a[232:235], v[80:83], v[204:207]\n\t"
        "v_mul_f32 v217, v217, v18\n\t"
        "v_mul_f32 v221, v221, v19\n\t"
        "v_mfma_f32_16x16x32_bf16 v[204:207], a[236:239], v[84:87], v[204:207]\n\t"
        "v_cvt_pk_bf16_f32 v189, v217, v221\n\t"
        "v_mul_f32 v218, v218, v18\n\t"
        "v_mfma_f32_16x16x32_bf16 v[204:207], v[232:235], v[88:91], v[204:207]\n\t"
        "v_mul_f32 v222, v222, v19\n\t"
        "v_cvt_pk_bf16_f32 v187, v218, v222\n\t"
        "v_mfma_f32_16x16x32_bf16 v[204:207], v[236:239], v[92:95], v[204:207]\n\t"
        "s_waitcnt lgkmcnt(1)\n\t"
        "s_barrier\n\t"
        "v_mfma_f32_16x16x32_bf16 v[196:199], v[228:231], v[92:95], v[196:199]\n\t"
        "buffer_store_dwordx2 v[184:185], v5, s[12:15], s90 offen offset:128 sc1\n\t"
        "v_mfma_f32_16x16x32_bf16 v[196:199], v[224:227], v[88:91], v[196:199]\n\t"
        "ds_read_b128 v[32:35], v4 offset:4096\n\t"
        "v_mul_f32 v219, v219, v18\n\t"
        "v_mfma_f32_16x16x32_bf16 v[196:199], a[228:231], v[84:87], v[196:199]\n\t"
        "ds_read_b128 v[40:43], v4 offset:6144\n\t"
        "v_mul_f32 v223, v223, v19\n\t"
        "v_mfma_f32_16x16x32_bf16 v[196:199], a[224:227], v[80:83], v[196:199]\n\t"
        "ds_read_b128 v[36:39], v4 offset:1024\n\t"
        "v_cvt_pk_bf16_f32 v191, v219, v223\n\t"
        "v_mfma_f32_16x16x32_bf16 v[128:131], a[0:3], v[44:47], 0\n\t"
        "ds_read_b128 v[20:23], v4 offset:3072\n\t"
        "v_lshl_or_b32 v65, v67, 4, v65\n\t"
        "v_mfma_f32_16x16x32_bf16 v[128:131], a[4:7], v[24:27], v[128:131]\n\t"
        "ds_read_b128 v[28:31], v4 offset:5120\n\t"
        "v_mad_u32_u16 v56, v65, s48, v1\n\t"
        "v_mad_u32_u16 v60, v65, s48, v1 op_sel:[1,0,0,0]\n\t"
        "v_lshl_or_b32 v68, v70, 4, v68\n\t"
        "v_mad_u32_u16 v49, v68, s48, v1\n\t"
        "v_mad_u32_u16 v53, v68, s48, v1 op_sel:[1,0,0,0]\n\t"
        "v_lshl_or_b32 v69, v71, 4, v69\n\t"
        "v_mad_u32_u16 v57, v69, s48, v1\n\t"
        "v_mad_u32_u16 v61, v69, s48, v1 op_sel:[1,0,0,0]\n\t"
        "s_waitcnt vmcnt(17) lgkmcnt(5)\n\t"
        "v_lshl_or_b32 v72, v74, 4, v72\n\t"
        "v_mad_u32_u16 v50, v72, s48, v1\n\t"
        "v_mad_u32_u16 v54, v72, s48, v1 op_sel:[1,0,0,0]\n\t"
        "v_lshl_or_b32 v73, v75, 4, v73\n\t"
        "v_mad_u32_u16 v58, v73, s48, v1\n\t"
        "v_mad_u32_u16 v62, v73, s48, v1 op_sel:[1,0,0,0]\n\t"
        "v_lshl_or_b32 v76, v78, 4, v76\n\t"
        "s_waitcnt lgkmcnt(3)\n\t"
        "v_mfma_f32_16x16x32_bf16 v[128:131], a[32:35], v[32:35], v[128:131]\n\t"
        "buffer_store_dwordx2 v[188:189], v5, s[12:15], s91 offen offset:128 sc1\n\t"
        "v_mfma_f32_16x16x32_bf16 v[128:131], a[36:39], v[40:43], v[128:131]\n\t"
        "ds_read_b128 v[92:95], v4 offset:7168\n\t"
        "v_mul_f32 v176, v176, v16\n\t"
        "v_mfma_f32_16x16x32_bf16 v[96:99], a[44:47], v[40:43], 0\n\t"
        "ds_read_b32 v56, v56\n\t"
        "v_mul_f32 v180, v180, v17\n\t"
        "v_mfma_f32_16x16x32_bf16 v[96:99], a[40:43], v[32:35], v[96:99]\n\t"
        "ds_read_b32 v60, v60\n\t"
        "v_cvt_pk_bf16_f32 v176, v176, v180\n\t"
        "v_mfma_f32_16x16x32_bf16 v[96:99], a[12:15], v[24:27], v[96:99]\n\t"
        "ds_read_b32 v49, v49\n\t"
        "v_mul_f32 v177, v177, v16\n\t"
        "v_mfma_f32_16x16x32_bf16 v[96:99], a[8:11], v[44:47], v[96:99]\n\t"
        "ds_read_b32 v53, v53\n\t"
        "s_waitcnt lgkmcnt(5)\n\t"
        "v_mfma_f32_16x16x32_bf16 v[100:103], a[8:11], v[36:39], 0\n\t"
        "ds_read_b128 v[88:91], v4 offset:8192\n\t"
        "v_mul_f32 v181, v181, v17\n\t"
        "v_mfma_f32_16x16x32_bf16 v[100:103], a[12:15], v[20:23], v[100:103]\n\t"
        "buffer_store_dwordx2 v[186:187], v5, s[12:15], s54 offen offset:128 sc1\n\t"
        "ds_read_b128 v[84:87], v4 offset:10240\n\t"
        "v_cvt_pk_bf16_f32 v180, v177, v181\n\t"
        "s_waitcnt lgkmcnt(6)\n\t"
        "v_mfma_f32_16x16x32_bf16 v[100:103], a[40:43], v[28:31], v[100:103]\n\t"
        "ds_read_b128 v[80:83], v4 offset:12288\n\t"
        "v_mul_f32 v178, v178, v16\n\t"
        "v_mfma_f32_16x16x32_bf16 v[100:103], a[44:47], v[92:95], v[100:103]\n\t"
        "ds_read_b128 v[40:43], v4 offset:14336\n\t"
        "v_mul_f32 v182, v182, v17\n\t"
        "v_mfma_f32_16x16x32_bf16 v[132:135], a[36:39], v[92:95], 0\n\t"
        "ds_read_b128 v[32:35], v4 offset:16384\n\t"
        "v_cvt_pk_bf16_f32 v178, v178, v182\n\t"
        "v_mfma_f32_16x16x32_bf16 v[132:135], a[32:35], v[28:31], v[132:135]\n\t"
        "ds_read_b128 v[24:27], v4 offset:18432\n\t"
        "v_mul_f32 v179, v179, v16\n\t"
        "v_mfma_f32_16x16x32_bf16 v[132:135], a[4:7], v[20:23], v[132:135]\n\t"
        "ds_read_b32 v57, v57\n\t"
        "v_mul_f32 v183, v183, v17\n\t"
        "v_mfma_f32_16x16x32_bf16 v[132:135], a[0:3], v[36:39], v[132:135]\n\t"
        "buffer_store_dwordx2 v[190:191], v5, s[12:15], s3 offen offset:128 sc1\n\t"
        "ds_read_b32 v61, v61\n\t"
        "s_waitcnt lgkmcnt(6)\n\t"
        "v_mfma_f32_16x16x32_bf16 v[128:131], a[64:67], v[88:91], v[128:131]\n\t"
        "ds_read_b32 v50, v50\n\t"
        "v_cvt_pk_bf16_f32 v182, v179, v183\n\t"
        "v_mfma_f32_16x16x32_bf16 v[128:131], a[68:71], v[84:87], v[128:131]\n\t"
        "ds_read_b32 v54, v54\n\t"
        "s_waitcnt lgkmcnt(6)\n\t"
        "v_mfma_f32_16x16x32_bf16 v[128:131], a[96:99], v[80:83], v[128:131]\n\t"
        "ds_read_b32 v58, v58\n\t"
        "v_mul_f32 v208, v208, v18\n\t"
        "v_mfma_f32_16x16x32_bf16 v[128:131], a[100:103], v[40:43], v[128:131]\n\t"
        "ds_read_b32 v62, v62\n\t"
        "s_waitcnt lgkmcnt(6)\n\t"
        "v_mfma_f32_16x16x32_bf16 v[128:131], a[128:131], v[32:35], v[128:131]\n\t"
        "ds_read_b128 v[44:47], v4 offset:9216\n\t"
        "v_mul_f32 v212, v212, v19\n\t"
        "v_mfma_f32_16x16x32_bf16 v[128:131], a[132:135], v[24:27], v[128:131]\n\t"
        "ds_read_b128 v[92:95], v4 offset:11264\n\t"
        "v_cvt_pk_bf16_f32 v177, v208, v212\n\t"
        "v_mfma_f32_16x16x32_bf16 v[96:99], a[140:143], v[24:27], v[96:99]\n\t"
        "buffer_store_dwordx2 v[176:177], v5, s[12:15], s70 offen offset:128 sc1\n\t"
        "v_mfma_f32_16x16x32_bf16 v[96:99], a[136:139], v[32:35], v[96:99]\n\t"
        "ds_read_b128 v[28:31], v4 offset:13312\n\t"
        "v_mul_f32 v209, v209, v18\n\t"
        "v_mfma_f32_16x16x32_bf16 v[96:99], a[108:111], v[40:43], v[96:99]\n\t"
        "ds_read_b128 v[20:23], v4 offset:15360\n\t"
        "v_mul_f32 v213, v213, v19\n\t"
        "v_mfma_f32_16x16x32_bf16 v[96:99], a[104:107], v[80:83], v[96:99]\n\t"
        "ds_read_b128 v[36:39], v4 offset:17408\n\t"
        "v_cvt_pk_bf16_f32 v181, v209, v213\n\t"
        "v_mfma_f32_16x16x32_bf16 v[96:99], a[76:79], v[84:87], v[96:99]\n\t"
        "ds_read_b128 v[24:27], v4 offset:19456\n\t"
        "v_mul_f32 v210, v210, v18\n\t"
        "v_mfma_f32_16x16x32_bf16 v[96:99], a[72:75], v[88:91], v[96:99]\n\t"
        "v_mul_f32 v214, v214, v19\n\t"
        "v_cvt_pk_bf16_f32 v179, v210, v214\n\t"
        "v_mul_f32 v211, v211, v18\n\t"
        "v_mul_f32 v215, v215, v19\n\t"
        "s_waitcnt lgkmcnt(4)\n\t"
        "v_mfma_f32_16x16x32_bf16 v[100:103], a[72:75], v[44:47], v[100:103]\n\t"
        "buffer_store_dwordx2 v[180:181], v5, s[12:15], s71 offen offset:128 sc1\n\t"
        "v_mfma_f32_16x16x32_bf16 v[100:103], a[76:79], v[92:95], v[100:103]\n\t"
        "v_cvt_pk_bf16_f32 v183, v211, v215\n\t"
        "v_mul_f32 v168, v168, v16\n\t"
        "v_mul_f32 v172, v172, v17\n\t"
        "v_cvt_pk_bf16_f32 v168, v168, v172\n\t"
        "v_mul_f32 v169, v169, v16\n\t"
        "s_waitcnt lgkmcnt(2)\n\t"
        "v_mfma_f32_16x16x32_bf16 v[100:103], a[104:107], v[28:31], v[100:103]\n\t"
        "v_mul_f32 v173, v173, v17\n\t"
        "v_cvt_pk_bf16_f32 v172, v169, v173\n\t"
        "v_mfma_f32_16x16x32_bf16 v[100:103], a[108:111], v[20:23], v[100:103]\n\t"
        "v_mul_f32 v170, v170, v16\n\t"
        "s_waitcnt lgkmcnt(0)\n\t"
        "v_mfma_f32_16x16x32_bf16 v[100:103], a[136:139], v[36:39], v[100:103]\n\t"
        "v_mul_f32 v174, v174, v17\n\t"
        "v_cvt_pk_bf16_f32 v170, v170, v174\n\t"
        "v_mfma_f32_16x16x32_bf16 v[100:103], a[140:143], v[24:27], v[100:103]\n\t"
        "v_mul_f32 v171, v171, v16\n\t"
        "v_mul_f32 v175, v175, v17\n\t"
        "v_mfma_f32_16x16x32_bf16 v[132:135], a[132:135], v[24:27], v[132:135]\n\t"
        "buffer_store_dwordx2 v[178:179], v5, s[12:15], s88 offen offset:128 sc1\n\t"
        "v_mfma_f32_16x16x32_bf16 v[132:135], a[128:131], v[36:39], v[132:135]\n\t"
        "v_cvt_pk_bf16_f32 v174, v171, v175\n\t"
        "v_mul_f32 v200, v200, v18\n\t"
        "v_mfma_f32_16x16x32_bf16 v[132:135], a[100:103], v[20:23], v[132:135]\n\t"
        "v_mul_f32 v204, v204, v19\n\t"
        "v_cvt_pk_bf16_f32 v169, v200, v204\n\t"
        "v_mfma_f32_16x16x32_bf16 v[132:135], a[96:99], v[28:31], v[132:135]\n\t"
        "v_mul_f32 v201, v201, v18\n\t"
        "v_mul_f32 v205, v205, v19\n\t"
        "v_mfma_f32_16x16x32_bf16 v[132:135], a[68:71], v[92:95], v[132:135]\n\t"
        "v_cvt_pk_bf16_f32 v173, v201, v205\n\t"
        "v_mad_u32_u16 v51, v76, s48, v1\n\t"
        "v_mfma_f32_16x16x32_bf16 v[132:135], a[64:67], v[44:47], v[132:135]\n\t"
        "ds_read_b32 v51, v51\n\t"
        "v_mad_u32_u16 v55, v76, s48, v1 op_sel:[1,0,0,0]\n\t"
        "v_lshl_or_b32 v77, v79, 4, v77\n\t"
        "v_mad_u32_u16 v59, v77, s48, v1\n\t"
        "v_mad_u32_u16 v63, v77, s48, v1 op_sel:[1,0,0,0]\n\t"
        "v_mul_f32 v202, v202, v18\n\t"
        "v_mul_f32 v206, v206, v19\n\t"
        "v_cvt_pk_bf16_f32 v171, v202, v206\n\t"
        "v_mul_f32 v203, v203, v18\n\t"
        "v_mul_f32 v207, v207, v19\n\t"
        "v_cvt_pk_bf16_f32 v175, v203, v207\n\t"
        "v_mul_f32 v160, v160, v16\n\t"
        "v_mul_f32 v164, v164, v17\n\t"
        "v_cvt_pk_bf16_f32 v160, v160, v164\n\t"
        "v_mul_f32 v161, v161, v16\n\t"
        "v_mul_f32 v165, v165, v17\n\t"
        "v_cvt_pk_bf16_f32 v164, v161, v165\n\t"
        "v_mul_f32 v162, v162, v16\n\t"
        "v_mul_f32 v166, v166, v17\n\t"
        "v_cvt_pk_bf16_f32 v162, v162, v166\n\t"
        "v_mul_f32 v163, v163, v16\n\t"
        "v_mul_f32 v167, v167, v17\n\t"
        "v_cvt_pk_bf16_f32 v166, v163, v167\n\t"
        "v_mul_f32 v192, v192, v18\n\t"
        "v_mul_f32 v196, v196, v19\n\t"
        "v_cvt_pk_bf16_f32 v161, v192, v196\n\t"
        "v_mul_f32 v193, v193, v18\n\t"
        "v_mul_f32 v197, v197, v19\n\t"
        "v_cvt_pk_bf16_f32 v165, v193, v197\n\t"
        "v_mul_f32 v194, v194, v18\n\t"
        "v_mul_f32 v198, v198, v19\n\t"
        "v_cvt_pk_bf16_f32 v163, v194, v198\n\t"
        "v_mul_f32 v195, v195, v18\n\t"
        "v_mul_f32 v199, v199, v19\n\t"
        "v_cvt_pk_bf16_f32 v167, v195, v199\n\t"
        "buffer_store_dwordx2 v[182:183], v5, s[12:15], s89 offen offset:128 sc1\n\t"
        "ds_read_b32 v55, v55\n\t"
        "ds_read_b32 v59, v59\n\t"
        "ds_read_b32 v63, v63\n\t"
        "s_waitcnt lgkmcnt(3)\n\t"
        "ds_write_b128 v3, v[48:51] offset:16384\n\t"
        "buffer_store_dwordx2 v[168:169], v5, s[12:15], s61 offen offset:128 sc1\n\t"
        "s_waitcnt lgkmcnt(3)\n\t"
        "ds_write_b128 v3, v[52:55] offset:17408\n\t"
        "s_waitcnt lgkmcnt(2)\n\t"
        "ds_write_b128 v3, v[56:59] offset:16400\n\t"
        "buffer_store_dwordx2 v[172:173], v5, s[12:15], s62 offen offset:128 sc1\n\t"
        "ds_write_b128 v3, v[60:63] offset:17424\n\t"
        "buffer_store_dwordx2 v[170:171], v5, s[12:15], s68 offen offset:128 sc1\n\t"
        "s_waitcnt lgkmcnt(0)\n\t"
        "s_barrier\n\t"
        "ds_read_b128 v[32:35], v4 offset:20480\n\t"
        "buffer_store_dwordx2 v[174:175], v5, s[12:15], s69 offen offset:128 sc1\n\t"
        "ds_read_b128 v[40:43], v4 offset:22528\n\t"
        "ds_read_b128 v[80:83], v4 offset:24576\n\t"
        "ds_read_b128 v[84:87], v4 offset:26624\n\t"
        "ds_read_b128 v[88:91], v4 offset:28672\n\t"
        "ds_read_b128 v[24:27], v4 offset:30720\n\t"
        "ds_read_b128 v[36:39], v4 offset:32768\n\t"
        "ds_read_b128 v[20:23], v4 offset:34816\n\t"
        "buffer_store_dwordx2 v[160:161], v5, s[12:15], s57 offen offset:128 sc1\n\t"
        "ds_read_b128 v[28:31], v4 offset:21504\n\t"
        "ds_read_b128 v[92:95], v4 offset:23552\n\t"
        "s_waitcnt lgkmcnt(8)\n\t"
        "v_mfma_f32_16x16x32_bf16 v[128:131], a[160:163], v[32:35], v[128:131]\n\t"
        "ds_read_b128 v[44:47], v4 offset:25600\n\t"
        "v_mfma_f32_16x16x32_bf16 v[128:131], a[164:167], v[40:43], v[128:131]\n\t"
        "ds_read_b128 v[64:67], v4 offset:27648\n\t"
        "s_waitcnt lgkmcnt(8)\n\t"
        "v_mfma_f32_16x16x32_bf16 v[128:131], a[192:195], v[80:83], v[128:131]\n\t"
        "ds_read_b128 v[68:71], v4 offset:29696\n\t"
        "v_mfma_f32_16x16x32_bf16 v[128:131], a[196:199], v[84:87], v[128:131]\n\t"
        "ds_read_b128 v[72:75], v4 offset:31744\n\t"
        "s_waitcnt lgkmcnt(8)\n\t"
        "v_mfma_f32_16x16x32_bf16 v[128:131], a[224:227], v[88:91], v[128:131]\n\t"
        "ds_read_b128 v[76:79], v4 offset:33792\n\t"
        "v_mfma_f32_16x16x32_bf16 v[128:131], a[228:231], v[24:27], v[128:131]\n\t"
        "buffer_store_dwordx2 v[164:165], v5, s[12:15], s58 offen offset:128 sc1\n\t"
        "s_waitcnt lgkmcnt(7)\n\t"
        "v_mfma_f32_16x16x32_bf16 v[128:131], v[224:227], v[36:39], v[128:131]\n\t"
        "v_mfma_f32_16x16x32_bf16 v[128:131], v[228:231], v[20:23], v[128:131]\n\t"
        ".Lp16_%=:\n\t"
        "s_cbranch_execz .Lret0_%=\n\t"
        "v_mfma_f32_16x16x32_bf16 v[96:99], v[236:239], v[20:23], v[96:99]\n\t"
        "ds_read_b128 v[20:23], v4 offset:35840\n\t"
        "v_mfma_f32_16x16x32_bf16 v[96:99], v[232:235], v[36:39], v[96:99]\n\t"
        "v_mfma_f32_16x16x32_bf16 v[96:99], a[236:239], v[24:27], v[96:99]\n\t"
        "v_mfma_f32_16x16x32_bf16 v[96:99], a[232:235], v[88:91], v[96:99]\n\t"
        "buffer_store_dwordx2 v[162:163], v5, s[12:15], s59 offen offset:128 sc1\n\t"
        "v_mfma_f32_16x16x32_bf16 v[96:99], a[204:207], v[84:87], v[96:99]\n\t"
        "v_mfma_f32_16x16x32_bf16 v[96:99], a[200:203], v[80:83], v[96:99]\n\t"
        "v_mfma_f32_16x16x32_bf16 v[96:99], a[172:175], v[40:43], v[96:99]\n\t"
        "v_mfma_f32_16x16x32_bf16 v[96:99], a[168:171], v[32:35], v[96:99]\n\t"
        "s_waitcnt lgkmcnt(0)\n\t"
        "v_mfma_f32_16x16x32_bf16 v[100:103], a[168:171], v[28:31], v[100:103]\n\t"
        "v_mfma_f32_16x16x32_bf16 v[100:103], a[172:175], v[92:95], v[100:103]\n\t"
        "v_mfma_f32_16x16x32_bf16 v[100:103], a[200:203], v[44:47], v[100:103]\n\t"
        "buffer_store_dwordx2 v[166:167], v5, s[12:15], s60 offen offset:128 sc1\n\t"
        "v_mfma_f32_16x16x32_bf16 v[100:103], a[204:207], v[64:67], v[100:103]\n\t"
        "v_mfma_f32_16x16x32_bf16 v[100:103], a[232:235], v[68:71], v[100:103]\n\t"
        "v_mfma_f32_16x16x32_bf16 v[100:103], a[236:239], v[72:75], v[100:103]\n\t"
        "v_mfma_f32_16x16x32_bf16 v[100:103], v[232:235], v[76:79], v[100:103]\n\t"
        "v_mfma_f32_16x16x32_bf16 v[100:103], v[236:239], v[20:23], v[100:103]\n\t"
        "v_mfma_f32_16x16x32_bf16 v[132:135], v[228:231], v[20:23], v[132:135]\n\t"
        "v_mfma_f32_16x16x32_bf16 v[132:135], v[224:227], v[76:79], v[132:135]\n\t"
        "v_mfma_f32_16x16x32_bf16 v[132:135], a[228:231], v[72:75], v[132:135]\n\t"
        "v_mul_f32 v96, v96, v11\n\t"
        "s_nop 3\n\t"
        "v_mul_f32 v100, v100, v0\n\t"
        "v_mfma_f32_16x16x32_bf16 v[132:135], a[224:227], v[68:71], v[132:135]\n\t"
        "v_cvt_pk_bf16_f32 v96, v96, v100\n\t"
        "v_mul_f32 v97, v97, v11\n\t"
        "v_mfma_f32_16x16x32_bf16 v[132:135], a[196:199], v[64:67], v[132:135]\n\t"
        "buffer_store_dword v96, v6, s[12:15], s61 offen sc1\n\t"
        "v_mfma_f32_16x16x32_bf16 v[132:135], a[192:195], v[44:47], v[132:135]\n\t"
        "v_mul_f32 v101, v101, v0\n\t"
        "v_cvt_pk_bf16_f32 v97, v97, v101\n\t"
        "v_mfma_f32_16x16x32_bf16 v[132:135], a[164:167], v[92:95], v[132:135]\n\t"
        "v_mul_f32 v98, v98, v11\n\t"
        "v_mul_f32 v102, v102, v0\n\t"
        "v_mfma_f32_16x16x32_bf16 v[132:135], a[160:163], v[28:31], v[132:135]\n\t"
        "v_cvt_pk_bf16_f32 v98, v98, v102\n\t"
        "v_mul_f32 v99, v99, v11\n\t"
        "v_mul_f32 v103, v103, v0\n\t"
        "v_cvt_pk_bf16_f32 v99, v99, v103\n\t"
        "buffer_store_dword v97, v6, s[12:15], s62 offen sc1\n\t"
        "v_mul_f32 v128, v128, v11\n\t"
        "s_nop 1\n\t"
        "v_mul_f32 v132, v132, v0\n\t"
        "v_cvt_pk_bf16_f32 v128, v128, v132\n\t"
        "v_mul_f32 v129, v129, v11\n\t"
        "v_mul_f32 v133, v133, v0\n\t"
        "v_cvt_pk_bf16_f32 v129, v129, v133\n\t"
        "v_mul_f32 v130, v130, v11\n\t"
        "v_mul_f32 v134, v134, v0\n\t"
        "v_cvt_pk_bf16_f32 v130, v130, v134\n\t"
        "v_mul_f32 v131, v131, v11\n\t"
        "v_mul_f32 v135, v135, v0\n\t"
        "v_cvt_pk_bf16_f32 v131, v131, v135\n\t"
        "buffer_store_dword v98, v6, s[12:15], s68 offen sc1\n\t"
        "buffer_store_dword v99, v6, s[12:15], s69 offen sc1\n\t"
        "buffer_store_dword v128, v6, s[12:15], s57 offen sc1\n\t"
        "buffer_store_dword v129, v6, s[12:15], s58 offen sc1\n\t"
        "buffer_store_dword v130, v6, s[12:15], s59 offen sc1\n\t"
        "buffer_store_dword v131, v6, s[12:15], s60 offen sc1\n\t"
        "s_endpgm\n\t"
        ".rept 300\n\t"
        "s_nop 0\n\t"
        ".endr\n\t"
        ".Lend%=:\n\t"
        :
        : "{s[0:1]}"(__builtin_amdgcn_kernarg_segment_ptr()), "{s2}"(blockIdx.x), "{v0}"(threadIdx.x)
        : "v1", "v2", "v3", "v4", "v5", "v6", "v7", "v8", "v9", "v10", "v11", "v12", "v13", "v14", "v15", "v16",
          "v17", "v18", "v19", "v20", "v21", "v22", "v23", "v24", "v25", "v26", "v27", "v28", "v29", "v30", "v31", "v32",
          "v33", "v34", "v35", "v36", "v37", "v38", "v39", "v40", "v41", "v42", "v43", "v44", "v45", "v46", "v47", "v48",
          "v49", "v50", "v51", "v52", "v53", "v54", "v55", "v56", "v57", "v58", "v59", "v60", "v61", "v62", "v63", "v64",
          "v65", "v66", "v67", "v68", "v69", "v70", "v71", "v72", "v73", "v74", "v75", "v76", "v77", "v78", "v79", "v80",
          "v81", "v82", "v83", "v84", "v85", "v86", "v87", "v88", "v89", "v90", "v91", "v92", "v93", "v94", "v95", "v96",
          "v97", "v98", "v99", "v100", "v101", "v102", "v103", "v104", "v105", "v106", "v107", "v108", "v109", "v110", "v111", "v112",
          "v113", "v114", "v115", "v116", "v117", "v118", "v119", "v120", "v121", "v122", "v123", "v124", "v125", "v126", "v127", "v128",
          "v129", "v130", "v131", "v132", "v133", "v134", "v135", "v136", "v137", "v138", "v139", "v140", "v141", "v142", "v143", "v144",
          "v145", "v146", "v147", "v148", "v149", "v150", "v151", "v152", "v153", "v154", "v155", "v156", "v157", "v158", "v159", "v160",
          "v161", "v162", "v163", "v164", "v165", "v166", "v167", "v168", "v169", "v170", "v171", "v172", "v173", "v174", "v175", "v176",
          "v177", "v178", "v179", "v180", "v181", "v182", "v183", "v184", "v185", "v186", "v187", "v188", "v189", "v190", "v191", "v192",
          "v193", "v194", "v195", "v196", "v197", "v198", "v199", "v200", "v201", "v202", "v203", "v204", "v205", "v206", "v207", "v208",
          "v209", "v210", "v211", "v212", "v213", "v214", "v215", "v216", "v217", "v218", "v219", "v220", "v221", "v222", "v223", "v224",
          "v225", "v226", "v227", "v228", "v229", "v230", "v231", "v232", "v233", "v234", "v235", "v236", "v237", "v238", "v239", "v240",
          "v241", "v242", "v243", "v244", "v245", "v246", "v247", "v248", "v249", "v250", "v251", "v252", "v253", "v254", "v255", "a0",
          "a1", "a2", "a3", "a4", "a5", "a6", "a7", "a8", "a9", "a10", "a11", "a12", "a13", "a14", "a15", "a16",
          "a17", "a18", "a19", "a20", "a21", "a22", "a23", "a24", "a25", "a26", "a27", "a28", "a29", "a30", "a31", "a32",
          "a33", "a34", "a35", "a36", "a37", "a38", "a39", "a40", "a41", "a42", "a43", "a44", "a45", "a46", "a47", "a48",
          "a49", "a50", "a51", "a52", "a53", "a54", "a55", "a56", "a57", "a58", "a59", "a60", "a61", "a62", "a63", "a64",
          "a65", "a66", "a67", "a68", "a69", "a70", "a71", "a72", "a73", "a74", "a75", "a76", "a77", "a78", "a79", "a80",
          "a81", "a82", "a83", "a84", "a85", "a86", "a87", "a88", "a89", "a90", "a91", "a92", "a93", "a94", "a95", "a96",
          "a97", "a98", "a99", "a100", "a101", "a102", "a103", "a104", "a105", "a106", "a107", "a108", "a109", "a110", "a111", "a112",
          "a113", "a114", "a115", "a116", "a117", "a118", "a119", "a120", "a121", "a122", "a123", "a124", "a125", "a126", "a127", "a128",
          "a129", "a130", "a131", "a132", "a133", "a134", "a135", "a136", "a137", "a138", "a139", "a140", "a141", "a142", "a143", "a144",
          "a145", "a146", "a147", "a148", "a149", "a150", "a151", "a152", "a153", "a154", "a155", "a156", "a157", "a158", "a159", "a160",
          "a161", "a162", "a163", "a164", "a165", "a166", "a167", "a168", "a169", "a170", "a171", "a172", "a173", "a174", "a175", "a176",
          "a177", "a178", "a179", "a180", "a181", "a182", "a183", "a184", "a185", "a186", "a187", "a188", "a189", "a190", "a191", "a192",
          "a193", "a194", "a195", "a196", "a197", "a198", "a199", "a200", "a201", "a202", "a203", "a204", "a205", "a206", "a207", "a208",
          "a209", "a210", "a211", "a212", "a213", "a214", "a215", "a216", "a217", "a218", "a219", "a220", "a221", "a222", "a223", "a224",
          "a225", "a226", "a227", "a228", "a229", "a230", "a231", "a232", "a233", "a234", "a235", "a236", "a237", "a238", "a239", "a240",
          "a241", "a242", "a243", "a244", "a245", "a246", "a247", "a248", "a249", "a250", "a251", "a252", "a253", "a254", "a255", "s3",
          "s4", "s5", "s6", "s7", "s8", "s9", "s10", "s11", "s12", "s13", "s14", "s15", "s16", "s17", "s18", "s19",
          "s20", "s21", "s22", "s23", "s24", "s25", "s26", "s27", "s28", "s29", "s30", "s31", "s33", "s34", "s35", "s36",
          "s37", "s38", "s39", "s40", "s41", "s42", "s43", "s44", "s45", "s46", "s47", "s48", "s49", "s50", "s51", "s52",
          "s53", "s54", "s55", "s56", "s57", "s58", "s59", "s60", "s61", "s62", "s63", "s64", "s65", "s66", "s67", "s68",
          "s69", "s70", "s71", "s72", "s73", "s74", "s75", "s76", "s77", "s78", "s79", "s80", "s81", "s82", "s83", "s84",
          "s85", "s86", "s87", "s88", "s89", "s90", "s91", "s92", "s93", "s94", "s95", "s96", "s97", "s98", "s99", "vcc",
          "scc", "memory");
    asm volatile("" :: "s"((unsigned)(size_t)(__attribute__((address_space(3))) char*)smem));
}

extern "C" void kernel_launch(void* const* d_in, const int* in_sizes, int n_in, void* d_out, int out_size,
                              void* d_ws, size_t ws_size, hipStream_t stream) {
    nf4_gemm_kernel<<<256, 256, 0, stream>>>(d_in[0], d_in[1], d_in[2], d_out);
}
```
